```python
import torch

def _softmax(x, axis=-1):
    return torch.softmax(x, dim=axis)
def _gelu(x):
    return torch.nn.functional.gelu(x, approximate="tanh")
def _silu(x):
    return torch.nn.functional.silu(x)
def _sigmoid(x):
    return torch.sigmoid(x)
def _layernorm(x, axis=-1, eps=1e-5):
    m = x.mean(dim=axis, keepdim=True)
    v = x.var(dim=axis, keepdim=True, unbiased=False)
    return (x - m) / torch.sqrt(v + eps)
def _scatter_add(tgt, idx, src, axis=0):
    return tgt.clone().index_add_(axis, idx, src)
def _segment_sum(src, seg, n):
    out = torch.zeros((n,) + src.shape[1:], dtype=src.dtype)
    return out.index_add_(0, seg, src)
def _pad(x, pw):
    flat = tuple(v for pair in reversed(pw) for v in pair)
    return torch.nn.functional.pad(x, flat)

kernel_name = 'gated_mix__softmax_full_mha__moe_arch_mod__prompt'

def setup_inputs():

    _w = (lambda a: (lambda t: t.to(torch.bfloat16) if t.is_floating_point() else t)(torch.as_tensor(a)))
    def _A_setup():
        B, S, D = 1, 2048, 448
        return {
            "x": _w((lambda *s: torch.randn(*s, dtype=torch.bfloat16) * 0.05)(B, S, D)),
            "wqkv": _w((lambda *s: torch.randn(*s, dtype=torch.bfloat16) * 1.6)(D, 3 * D)),
            "wo": _w((lambda *s: torch.randn(*s, dtype=torch.bfloat16) * 0.1)(D, D)),
        }
    _A_in = _A_setup()

    def _B_setup():
        B, S, D, F, C = 1, 2048, 448, 1792, 8
        return {
            "x": _w((lambda *s: torch.randn(*s, dtype=torch.bfloat16) * 0.05)(B, S, D)),
            "w_r": _w((lambda *s: torch.randn(*s, dtype=torch.bfloat16) * 3.2)(D)),
            "w_gate": _w((lambda *s: torch.randn(*s, dtype=torch.bfloat16) * 0.4)(D, F)),
            "w_up": _w((lambda *s: torch.randn(*s, dtype=torch.bfloat16) * 0.4)(D, F)),
            "w_down": _w((lambda *s: torch.randn(*s, dtype=torch.bfloat16) * 0.4)(F, D)),
        }
    _B_in = _B_setup()

    return {
        "A_x": _A_in["x"],
        "A_wqkv": _A_in["wqkv"],
        "A_wo": _A_in["wo"],
        "B_x": _B_in["x"],
        "B_w_r": _B_in["w_r"],
        "B_w_gate": _B_in["w_gate"],
        "B_w_up": _B_in["w_up"],
        "B_w_down": _B_in["w_down"],
        "gate_w": _w((lambda *s: torch.randn(*s, dtype=torch.bfloat16) * 0.05)(448) * 16.0),
    }

def reference(A_x, A_wqkv, A_wo, B_x, B_w_r, B_w_gate, B_w_up, B_w_down, gate_w):
    def _A_ref(x, wqkv, wo):
        B, S, D = x.shape
        H = 16; Dh = D // H
        qkv = (lambda x, *d: x.permute(*d))((x @ wqkv).reshape(B, S, 3, H, Dh), 2, 0, 3, 1, 4)
        q, k, v = qkv[0], qkv[1], qkv[2]
        scale = Dh ** -0.5
        scores = torch.einsum("bhqd,bhkd->bhqk", q, k) * scale
        p = _softmax(scores, axis=-1)
        o = torch.einsum("bhqk,bhkd->bhqd", p, v)
        return (lambda x, *d: x.permute(*d))(o, 0, 2, 1, 3).reshape(B, S, D) @ wo

    def _B_ref(x, w_r, w_gate, w_up, w_down):
        B, S, D = x.shape
        C = 8
        r = x @ w_r
        idx = torch.argsort(-r, axis=-1)[:, :C]
        idx_e = (lambda x, n, axis: torch.repeat_interleave(x, n, dim=axis))(idx.reshape(B, C, 1), D, axis=2)
        x_sel = (lambda x, i, axis: torch.take_along_dim(x, i, dim=axis))(x, idx_e, axis=1)
        g = _silu(x_sel @ w_gate)
        u = x_sel @ w_up
        y = (g * u) @ w_down
        r_sel = _sigmoid((lambda x, i, axis: torch.take_along_dim(x, i, dim=axis))(r, idx, axis=1))
        y = y * r_sel.reshape(B, C, 1)
        b_off = (torch.arange(B) * S).reshape(B, 1)
        flat_idx = (idx + b_off).reshape(B * C)
        zero = (x * 0.0).reshape(B * S, D)
        upd = _scatter_add(zero, flat_idx, y.reshape(B * C, D), axis=0)
        return x + upd.reshape(B, S, D)

    x = A_x
    g = _sigmoid((x * gate_w).sum(-1, keepdims=True))
    a = _A_ref(x, A_wqkv, A_wo)
    b = _B_ref(x, B_w_r, B_w_gate, B_w_up, B_w_down)
    return g * a + (1 - g) * b
```

```cpp
#include <hip/hip_runtime.h>
#include <hip/hip_bf16.h>
#include <stdint.h>

typedef short bf16x8 __attribute__((ext_vector_type(8)));
typedef short s16x4 __attribute__((ext_vector_type(4)));
typedef float f32x16 __attribute__((ext_vector_type(16)));
typedef float f32x4 __attribute__((ext_vector_type(4)));
typedef unsigned u32x4 __attribute__((ext_vector_type(4)));
typedef unsigned u32x2 __attribute__((ext_vector_type(2)));
#define LAS __attribute__((address_space(3)))
#define GAS __attribute__((address_space(1)))
typedef LAS char* lptr;
#define MFMA32(a, b, c) __builtin_amdgcn_mfma_f32_32x32x16_bf16(a, b, c, 0, 0, 0)
#define SBAR() __builtin_amdgcn_sched_barrier(0)
#define PIN(x) asm volatile("" : "+v"(x))
#define DEVI __device__ __forceinline__

constexpr int S_ = 2048, D_ = 448, F_ = 1792;
constexpr int XROW = 896, WROW = 2688, FROW = 3584;
constexpr size_t WS_KV = 0, WS_Q = 4u << 20, WS_O = 6u << 20, WS_G = 8u << 20, WS_CAND = WS_G + 8192, WS_SEL = WS_G + 12288, WS_PART = WS_G + 65536;
constexpr int NMOD = 56;
constexpr float QSCALE = 0.18898223650461362f * 1.4426950408889634f;

#define STAMP(ws, k)

typedef __bf16 bf16x2_t __attribute__((ext_vector_type(2)));
typedef float f32x2 __attribute__((ext_vector_type(2)));
DEVI unsigned cvtpk(float lo, float hi) { const f32x2 v = {lo, hi}; return __builtin_bit_cast(unsigned, __builtin_convertvector(v, bf16x2_t)); }
DEVI float bflo(unsigned u) { return __uint_as_float(u << 16); }
DEVI float bfhi(unsigned u) { return __uint_as_float(u & 0xffff0000u); }
DEVI float rbf(float f) { return bflo(cvtpk(f, f)); }
DEVI void glds16(unsigned voff, const void* sbase, unsigned lds) {
    unsigned sv;
    asm volatile("s_mov_b32 %0, m0\n\ts_mov_b32 m0, %3\n\ts_nop 0\n\tglobal_load_lds_dwordx4 %1, %2\n\ts_mov_b32 m0, %0" : "=&s"(sv) : "v"(voff), "s"(sbase), "s"(lds) : "memory");
}
DEVI bf16x8 ldsr(lptr p) { return *(const LAS bf16x8*)p; }
DEVI s16x4 ldtr(lptr p) { return __builtin_amdgcn_ds_read_tr16_b64_v4i16((LAS s16x4*)p); }
DEVI bf16x8 ldtr2(lptr p0, lptr p1) { s16x4 a = ldtr(p0), b = ldtr(p1); return (bf16x8){a[0], a[1], a[2], a[3], b[0], b[1], b[2], b[3]}; }
#define WAITV(N) asm volatile("s_waitcnt vmcnt(" #N ")" ::: "memory")
#define WAIT_BAR(N) asm volatile("s_waitcnt vmcnt(" #N ") lgkmcnt(0)\n\ts_barrier" ::: "memory")
#define LBAR() asm volatile("s_waitcnt lgkmcnt(0)\n\ts_barrier" ::: "memory")
DEVI void swap32(unsigned& a, unsigned& b) { auto r = __builtin_amdgcn_permlane32_swap(a, b, false, false); a = r[0]; b = r[1]; }
DEVI void ct_to_chunks(const f32x16& c, float sc, u32x4& p0, u32x4& p1) {
    unsigned a0 = cvtpk(c[0] * sc, c[1] * sc), a1 = cvtpk(c[2] * sc, c[3] * sc), b0 = cvtpk(c[4] * sc, c[5] * sc), b1 = cvtpk(c[6] * sc, c[7] * sc);
    unsigned c0 = cvtpk(c[8] * sc, c[9] * sc), c1 = cvtpk(c[10] * sc, c[11] * sc), d0 = cvtpk(c[12] * sc, c[13] * sc), d1 = cvtpk(c[14] * sc, c[15] * sc);
    swap32(a0, b0); swap32(a1, b1); swap32(c0, d0); swap32(c1, d1);
    p0 = (u32x4){a0, a1, b0, b1}; p1 = (u32x4){c0, c1, d0, d1};
}
DEVI void blk_map(int b, int& h, int& qb) { const int xcd = b & 7, i = b >> 3; h = (xcd & 3) * 4 + (i & 3); qb = (xcd >> 2) * 8 + (i >> 2); }
DEVI unsigned rank128(lptr keys, int tid, unsigned& mykey) {
    mykey = *(const LAS unsigned*)(keys + (tid >> 2) * 4); unsigned cnt = 0;
#pragma unroll
    for (int j = 0; j < 8; ++j) { const u32x4 v = *(const LAS u32x4*)(keys + (tid & 3) * 128 + j * 16); cnt += (v[0] > mykey) + (v[1] > mykey) + (v[2] > mykey) + (v[3] > mykey); }
    cnt += __builtin_amdgcn_update_dpp(0u, cnt, 0xB1, 0xf, 0xf, true);
    cnt += __builtin_amdgcn_update_dpp(0u, cnt, 0x4E, 0xf, 0xf, true);
    return cnt;
}

constexpr int L1_W = 114688, L1_BYTES = 163840;
__global__ __launch_bounds__(512) void k_qkv(const char* __restrict__ x, const char* __restrict__ wqkv, const char* __restrict__ wr, const char* __restrict__ gw, char* __restrict__ ws) {
    extern __shared__ __attribute__((aligned(16))) char lds_[];
    STAMP(ws, 0);
    const lptr lds = (lptr)lds_; const unsigned lds0 = (unsigned)(uintptr_t)lds_;
    int h, qb; blk_map(blockIdx.x, h, qb);
    const int tid = threadIdx.x, lane = tid & 63, r = lane & 31, hh = lane >> 5; const int wid = __builtin_amdgcn_readfirstlane(tid >> 6), rt = wid & 3, ct = wid >> 2;
    const unsigned xoff = (unsigned)((8 * wid + (lane >> 3)) * XROW + (((lane & 7) ^ ((4 * wid + (lane >> 4)) & 7)) * 16));
    const char* xb = x + (size_t)qb * 128 * XROW;
    const unsigned woff = (unsigned)((8 * wid + (lane >> 3)) * WROW + (((lane & 7) ^ (((lane >> 4) & 1) << 2)) * 16));
    const char* wb = wqkv + 896 + h * 128;
    const unsigned qoff = (unsigned)((16 * wid + (lane >> 2)) * WROW + (lane & 3) * 16);
    const char* wq = wqkv + h * 56 - (h & 1) * 8;
    const lptr xr = lds + (rt * 32 + r) * 128; const int swz = (r >> 1) & 7;
    const int tq = (lane & 15) >> 2, tp = lane & 3, tg = (lane >> 4) & 1;
    const lptr wtr = lds + L1_W + (8 * hh + tq) * 128 + ((ct ^ (tq >> 1)) * 64) + 32 * tg + 8 * tp;
    const lptr qtr = lds + L1_W + 8192 + (8 * hh + tq) * 64 + 32 * tg + 8 * tp;
#define XDMA(kc) do { glds16(xoff, xb + (kc) * 128, lds0 + (kc) * 16384 + wid * 1024); glds16(xoff, xb + 64 * XROW + (kc) * 128, lds0 + (kc) * 16384 + 8192 + wid * 1024); } while (0)
#define WDMA(kc, slot) glds16(woff, wb + (kc) * 64 * WROW, lds0 + L1_W + (slot) * 8192 + wid * 1024)
#define QDMA(j) glds16(qoff, wq + (j) * 128 * WROW, lds0 + L1_W + 8192 + (j) * 8192 + wid * 1024)
#define XFRAG(kc, s4) ldsr(xr + (kc) * 16384 + (((2 * (s4) + hh) ^ swz) * 16))
    if (h < 14) {
#pragma unroll
        for (int kc = 0; kc < 6; ++kc) { XDMA(kc); WDMA(kc, kc); }
        XDMA(6);
        f32x16 acc = {};
        const bool isK = h < 7;
#define KSTEP(kc, slot) do { if (isK) { _Pragma("unroll") for (int s4 = 0; s4 < 4; ++s4) acc = MFMA32(ldtr2(wtr + (slot) * 8192 + s4 * 2048, wtr + (slot) * 8192 + s4 * 2048 + 512), XFRAG(kc, s4), acc); } \
                             else     { _Pragma("unroll") for (int s4 = 0; s4 < 4; ++s4) acc = MFMA32(XFRAG(kc, s4), ldtr2(wtr + (slot) * 8192 + s4 * 2048, wtr + (slot) * 8192 + s4 * 2048 + 512), acc); } } while (0)
        WAIT_BAR(17); KSTEP(0, 0);
        WAIT_BAR(14); WDMA(6, 0); KSTEP(1, 1);
        WAIT_BAR(12); QDMA(0); KSTEP(2, 2);
        WAIT_BAR(10); QDMA(1); KSTEP(3, 3);
        WAIT_BAR(8); QDMA(2); KSTEP(4, 4);
        WAIT_BAR(6); if (wid < 4) QDMA(3); KSTEP(5, 5);
        WAIT_BAR(3); KSTEP(6, 0);
        STAMP(ws, 1);
        char* kv = ws + WS_KV + (size_t)(qb * 4 + rt) * 4096;
        if (isK) {
            u32x4 p0, p1; ct_to_chunks(acc, 1.f, p0, p1);
#pragma unroll
            for (int s = 0; s < 2; ++s) {
                const int cc = h * 8 + ct * 4 + 2 * s + hh, hp = cc / 7, u = cc - hp * 7;
                const int head = 2 * hp + (u > 3), piece = u > 3 ? u - 3 : u;
                const u32x4 v = s ? p1 : p0;
                *(u32x4*)(kv + (size_t)head * 262144 + (piece >> 1) * 1024 + ((piece & 1) * 32 + r) * 16) = v;
                if (u == 3) *(u32x4*)(kv + (size_t)(head + 1) * 262144 + r * 16) = v;
            }
        } else {
            const int gv = (h - 7) * 64 + ct * 32 + r, head = gv / 28, d = gv - head * 28;
            char* dst = kv + (size_t)head * 262144 + 2048 + (hh * 32 + d) * 16;
            *(u32x4*)dst = (u32x4){cvtpk(acc[0], acc[1]), cvtpk(acc[2], acc[3]), cvtpk(acc[4], acc[5]), cvtpk(acc[6], acc[7])};
            *(u32x4*)(dst + 1024) = (u32x4){cvtpk(acc[8], acc[9]), cvtpk(acc[10], acc[11]), cvtpk(acc[12], acc[13]), cvtpk(acc[14], acc[15])};
            if (d == 27) { const u32x4 one = {0x3F803F80u, 0x3F803F80u, 0x3F803F80u, 0x3F803F80u}; *(u32x4*)(dst + 16) = one; *(u32x4*)(dst + 1040) = one; }
        }
        WAIT_BAR(0);
    } else {
#pragma unroll
        for (int kc = 0; kc < 7; ++kc) XDMA(kc);
        if (h == 14 && wid < 2 && lane < 56) glds16(lane * 16, wid ? gw : wr, lds0 + L1_W + wid * 1024);
        QDMA(0); QDMA(1); QDMA(2); if (wid < 4) QDMA(3);
        WAIT_BAR(0);
        if (h == 14) {
            if (wid >= 4) {
                f32x16 acc = {};
                const lptr ar = lds + L1_W + (r & 1) * 1024 + hh * 16;
#pragma unroll
                for (int s = 0; s < 28; ++s) acc = MFMA32(ldsr(ar + s * 32), XFRAG(s >> 2, s & 3), acc);
                if (hh == 0) {
                    const int tok = qb * 128 + rt * 32 + r;
                    const unsigned u = cvtpk(acc[0], acc[0]) & 0xffffu;
                    unsigned o = (u & 0x8000u) ? (~u & 0xffffu) : (u | 0x8000u); if ((u & 0x7fffu) > 0x7f80u) o = 0;
                    *(LAS unsigned*)(lds + L1_W + 2048 + (rt * 32 + r) * 4) = (o << 16) | (unsigned)(0xffff - tok);
                    const float gl = rbf(acc[1]);
                    ((float*)(ws + WS_G))[tok] = rbf(1.f / (1.f + __expf(-gl)));
                }
            }
            LBAR();
            unsigned key; const unsigned rk = rank128(lds + L1_W + 2048, tid, key);
            if ((tid & 3) == 0 && rk < 8) ((unsigned*)(ws + WS_CAND))[qb * 8 + rk] = key;
        }
    }
    if (wid < 4) {
        f32x16 acc = {};
#pragma unroll
        for (int s = 0; s < 28; ++s) acc = MFMA32(ldtr2(qtr + s * 1024, qtr + s * 1024 + 256), XFRAG(s >> 2, s & 3), acc);
        u32x4 p0, p1; ct_to_chunks(acc, QSCALE, p0, p1);
        if (h & 1) { if (hh == 0) { p0[0] = 0; p0[1] = 0; } } else { if (hh == 1) { p1[2] = 0; p1[3] = 0; } }
        char* q = ws + WS_Q + (size_t)(h * 64 + qb * 4 + rt) * 2048 + lane * 16;
        *(u32x4*)q = p0; *(u32x4*)(q + 1024) = p1;
    }
    STAMP(ws, 2);
#undef XDMA
#undef WDMA
#undef QDMA
#undef KSTEP
}

DEVI void mod_block(int mb, const char* __restrict__ x, const char* __restrict__ wg, const char* __restrict__ wu, const char* __restrict__ wd, char* __restrict__ ws, char* lds_) {
    const lptr lds = (lptr)lds_; const unsigned lds0 = (unsigned)(uintptr_t)lds_;
    const int tid = threadIdx.x, lane = tid & 63, r = lane & 31, hh = lane >> 5; const int wid = __builtin_amdgcn_readfirstlane(tid >> 6);
    constexpr int M_G = 0, M_U = 28672, M_X = 57344, M_RED = M_X + 8 * 1040, M_KEY = M_RED + 8192, M_SEL = M_KEY + 512, M_H = M_SEL + 64;
    const unsigned goff = (unsigned)((lane >> 2) * FROW + (lane & 3) * 16);
#pragma unroll
    for (int j = 0; j < 7; ++j) { const int p = wid + 8 * j, m = p >= 28, pr = p - 28 * m; glds16(goff, (m ? wu : wg) + (size_t)pr * 16 * FROW + mb * 64, lds0 + p * 1024); }
    if (tid < 128) *(LAS unsigned*)(lds + M_KEY + tid * 4) = ((const unsigned*)(ws + WS_CAND))[tid];
    LBAR();
    unsigned key; const unsigned rk = rank128(lds + M_KEY, tid, key);
    if ((tid & 3) == 0 && rk < 8) { *(LAS unsigned*)(lds + M_SEL + rk * 4) = key; if (mb == 0) ((unsigned*)(ws + WS_SEL))[rk] = key; }
    LBAR();
    { const unsigned k = __builtin_amdgcn_readfirstlane(*(const LAS unsigned*)(lds + M_SEL + wid * 4)); const int tok = 0xffff - (k & 0xffff);
      if (lane < 56) glds16(lane * 16, x + (size_t)tok * XROW, lds0 + M_X + wid * 1040); }
    WAIT_BAR(0);
    const int tq = (lane & 15) >> 2, tp = lane & 3, tg = (lane >> 4) & 1;
    { const int m = wid & 1, kq = wid >> 1;
      const lptr wt = lds + m * M_U + (kq * 112 + 8 * hh + tq) * 64 + 32 * tg + 8 * tp; const lptr xs = lds + M_X + (r & 7) * 1040 + kq * 224 + hh * 16;
      f32x16 acc = {};
#pragma unroll
      for (int s = 0; s < 7; ++s) acc = MFMA32(ldtr2(wt + s * 1024, wt + s * 1024 + 256), ldsr(xs + s * 32), acc);
      if (r < 8) {
#pragma unroll
          for (int a = 0; a < 4; ++a) *(LAS f32x4*)(lds + M_RED + wid * 1024 + r * 128 + (8 * a + 4 * hh) * 4) = (f32x4){acc[4 * a], acc[4 * a + 1], acc[4 * a + 2], acc[4 * a + 3]};
      } }
    LBAR();
#pragma unroll
    for (int j = 0; j < 4; ++j) { const int row = wid * 4 + j; if (lane < 56) glds16(lane * 16, wd + (size_t)(mb * 32 + row) * XROW, lds0 + row * 1088); }
    if (tid < 256) {
        float gv = 0.f, uv = 0.f;
#pragma unroll
        for (int kq = 0; kq < 4; ++kq) { gv += *(const LAS float*)(lds + M_RED + (2 * kq) * 1024 + tid * 4); uv += *(const LAS float*)(lds + M_RED + (2 * kq + 1) * 1024 + tid * 4); }
        gv = rbf(gv); const float sg = rbf(gv / (1.f + __expf(-gv)));
        *(LAS unsigned short*)(lds + M_H + (tid >> 5) * 80 + (tid & 31) * 2) = (unsigned short)cvtpk(sg * rbf(uv), 0.f);
    }
    WAIT_BAR(0);
    const bf16x8 h0 = ldsr(lds + M_H + (r & 7) * 80 + hh * 16), h1 = ldsr(lds + M_H + (r & 7) * 80 + 32 + hh * 16);
    float* part = (float*)(ws + WS_PART) + (size_t)mb * 8 * D_;
#pragma unroll
    for (int j = 0; j < 2; ++j) { const int nt = wid + 8 * j;
        if (nt < 14) { const lptr wt = lds + (8 * hh + tq) * 1088 + nt * 64 + 32 * tg + 8 * tp;
            f32x16 acc = {}; acc = MFMA32(ldtr2(wt, wt + 4 * 1088), h0, acc); acc = MFMA32(ldtr2(wt + 16 * 1088, wt + 20 * 1088), h1, acc);
            if (r < 8) {
#pragma unroll
                for (int a = 0; a < 4; ++a) *(f32x4*)(part + r * D_ + nt * 32 + 8 * a + 4 * hh) = (f32x4){acc[4 * a], acc[4 * a + 1], acc[4 * a + 2], acc[4 * a + 3]};
            } } }
}

constexpr int NSLOT = 4, L2_BYTES = 76800;
#define MX3(a, b, c) __builtin_fmaxf(__builtin_fmaxf((a), (b)), (c))
DEVI float max16(const f32x16& s) {
    float a = MX3(s[0], s[1], s[2]), b = MX3(s[3], s[4], s[5]); a = MX3(a, s[6], s[7]); b = MX3(b, s[8], s[9]); a = MX3(a, s[10], s[11]); b = MX3(b, s[12], s[13]); a = MX3(a, s[14], s[15]);
    return __builtin_fmaxf(a, b);
}
DEVI float halfmax(float m) { unsigned a = __float_as_uint(m), b = a; swap32(a, b); return __builtin_fmaxf(__uint_as_float(a), __uint_as_float(b)); }

__global__ __launch_bounds__(512) void k_attn(const char* __restrict__ x, const char* __restrict__ wg, const char* __restrict__ wu, const char* __restrict__ wd, char* __restrict__ ws) {
    extern __shared__ __attribute__((aligned(16))) char lds_[];
    if (blockIdx.x >= 256) { mod_block(blockIdx.x - 256, x, wg, wu, wd, ws, lds_); return; }
    STAMP(ws, 3);
    const lptr lds = (lptr)lds_; const unsigned lds0 = (unsigned)(uintptr_t)lds_;
    int h, qb; blk_map(blockIdx.x, h, qb);
    const int tid = threadIdx.x, lane = tid & 63, hh = lane >> 5; const int wid = __builtin_amdgcn_readfirstlane(tid >> 6), qt = wid & 3, kg = wid >> 2;
    const char* kv = ws + WS_KV + (size_t)h * 262144;
    const unsigned koff = wid * 2048 + lane * 16;
#define KVDMA(i) do { const unsigned dst_ = lds0 + ((i) & (NSLOT - 1)) * 16384 + wid * 2048; glds16(koff, kv + (i) * 16384, dst_); glds16(koff, kv + (i) * 16384 + 1024, dst_ + 1024); } while (0)
    const char* qp = ws + WS_Q + (size_t)(h * 64 + qb * 4 + qt) * 2048 + lane * 16;
    bf16x8 q0 = *(const bf16x8*)qp, q1 = *(const bf16x8*)(qp + 1024);
    PIN(q0); PIN(q1);
    const lptr rd = lds + kg * 8192 + lane * 16;
    bool have_m = false; float m = 0.f; f32x16 o;
    for (;;) {
        KVDMA(0); KVDMA(1); KVDMA(2);
        WAIT_BAR(2);
        bf16x8 k0 = ldsr(rd), k1 = ldsr(rd + 1024), v0, v1;
        f32x16 sA, sB; const f32x16 zero = {};
        sA = MFMA32(k0, q0, zero); sA = MFMA32(k1, q1, sA);
        k0 = ldsr(rd + 4096); k1 = ldsr(rd + 4096 + 1024);
        if (!have_m) m = halfmax(max16(sA));
        f32x16 negm;
#pragma unroll
        for (int i = 0; i < 16; ++i) { sA[i] -= m; negm[i] = -m; }
        PIN(negm);
        o = (f32x16){};
        u32x4 p0, p1;
#define EX(v) __builtin_amdgcn_exp2f(v)
#define E4(X, i, P, j) do { X[i] = EX(X[i]); X[i + 1] = EX(X[i + 1]); X[i + 2] = EX(X[i + 2]); X[i + 3] = EX(X[i + 3]); P[j] = cvtpk(X[i], X[i + 1]); P[j + 1] = cvtpk(X[i + 2], X[i + 3]); PIN(P); SBAR(); } while (0)
#define TILE(C, N, VP, KP, HASN, HASK) do { \
        v0 = ldsr(VP); v1 = ldsr((VP) + 1024); SBAR(); \
        if (HASN) { N = MFMA32(k0, q0, negm); SBAR(); } \
        E4(C, 0, p0, 0); \
        if (HASN) { N = MFMA32(k1, q1, N); SBAR(); } \
        if (HASK) { k0 = ldsr(KP); k1 = ldsr((KP) + 1024); SBAR(); } \
        E4(C, 4, p0, 2); \
        E4(C, 8, p1, 0); \
        o = MFMA32(v0, __builtin_bit_cast(bf16x8, p0), o); SBAR(); \
        E4(C, 12, p1, 2); \
        o = MFMA32(v1, __builtin_bit_cast(bf16x8, p1), o); SBAR(); \
        } while (0)
        for (int i = 0; i < 15; ++i) {
            if (i > 0) { if (i < 14) WAIT_BAR(2); else WAIT_BAR(0); }
            if (i < 13) KVDMA(i + 3);
            const lptr cur = rd + (i & (NSLOT - 1)) * 16384, nxt = rd + ((i + 1) & (NSLOT - 1)) * 16384;
            TILE(sA, sB, cur + 2048, nxt, true, true);
            TILE(sB, sA, cur + 4096 + 2048, nxt + 4096, true, true);
        }
        { const lptr cur = rd + (15 & (NSLOT - 1)) * 16384;
          TILE(sA, sB, cur + 2048, cur, true, false);
          TILE(sB, sA, cur + 4096 + 2048, cur, false, false); }
        const bool bad = !have_m && hh == 1 && !(o[12] <= 1.0e18f);
        LBAR();
        if (tid == 0) *(LAS unsigned*)(lds + 65536) = 0;
        LBAR();
        if (bad) *(LAS unsigned*)(lds + 65536) = 1;
        LBAR();
        if (__builtin_expect(__builtin_amdgcn_readfirstlane(*(const LAS unsigned*)(lds + 65536)) == 0, 1)) break;
        float mm = -INFINITY;
        for (int n = 0; n < 32; ++n) { const char* kp = kv + ((n >> 1) * 4 + 2 * kg + (n & 1)) * 4096 + lane * 16;
            f32x16 s = MFMA32(*(const bf16x8*)kp, q0, zero); s = MFMA32(*(const bf16x8*)(kp + 1024), q1, s); mm = __builtin_fmaxf(mm, max16(s)); }
        m = halfmax(mm); have_m = true;
    }
    STAMP(ws, 4);
    const lptr cb = lds + qt * 5120 + lane * 16;
    if (kg == 1) {
#pragma unroll
        for (int a = 0; a < 4; ++a) *(LAS f32x4*)(cb + a * 1024) = (f32x4){o[4 * a], o[4 * a + 1], o[4 * a + 2], o[4 * a + 3]};
        *(LAS float*)(lds + qt * 5120 + 4096 + lane * 4) = m;
    }
    LBAR();
    if (kg == 0) {
        const float m2 = *(const LAS float*)(lds + qt * 5120 + 4096 + lane * 4), mx = __builtin_fmaxf(m, m2);
        const float f1 = EX(m - mx), f2 = EX(m2 - mx);
#pragma unroll
        for (int a = 0; a < 4; ++a) { const f32x4 t = *(const LAS f32x4*)(cb + a * 1024);
#pragma unroll
            for (int j = 0; j < 4; ++j) o[4 * a + j] = o[4 * a + j] * f1 + t[j] * f2; }
        unsigned la = __float_as_uint(o[12]), lb = la; swap32(la, lb);
        const float inv = 1.f / __uint_as_float(lb);
        u32x4 w0 = {cvtpk(o[0] * inv, o[1] * inv), cvtpk(o[2] * inv, o[3] * inv), cvtpk(o[4] * inv, o[5] * inv), cvtpk(o[6] * inv, o[7] * inv)};
        u32x4 w1 = {cvtpk(o[8] * inv, o[9] * inv), cvtpk(o[10] * inv, o[11] * inv), cvtpk(o[12] * inv, o[13] * inv), cvtpk(o[14] * inv, o[15] * inv)};
        if (hh) { w1[2] = 0; w1[3] = 0; }
        char* op = ws + WS_O + (size_t)((qb * 4 + qt) * 16 + h) * 2048 + lane * 16;
        *(u32x4*)op = w0; *(u32x4*)(op + 1024) = w1;
    }
    STAMP(ws, 5);
}

constexpr int L3_O = 114688, L3_BYTES = L3_O + 32768 + 8192;
__global__ __launch_bounds__(512) void k_out(const char* __restrict__ x, const char* __restrict__ wo, char* __restrict__ ws, char* __restrict__ out) {
    extern __shared__ __attribute__((aligned(16))) char lds_[];
    STAMP(ws, 6);
    const lptr lds = (lptr)lds_; const unsigned lds0 = (unsigned)(uintptr_t)lds_;
    int h, qb; blk_map(blockIdx.x, h, qb);
    const int qtile = qb * 4 + (h >> 2), cg = h & 3;
    const int tid = threadIdx.x, lane = tid & 63, r = lane & 31, hh = lane >> 5; const int wid = __builtin_amdgcn_readfirstlane(tid >> 6), ct = wid & 3, kh = wid >> 2;
#pragma unroll
    for (int j = 0; j < 4; ++j) glds16(wid * 4096 + lane * 16, ws + WS_O + (size_t)qtile * 32768 + j * 1024, lds0 + L3_O + wid * 4096 + j * 1024);
    { const int c = (lane & 15) ^ (4 * (lane >> 4)); const unsigned off = (unsigned)((4 * wid + (lane >> 4)) * XROW + c * 16);
      if (cg < 3 || c < 8) {
#pragma unroll
          for (int j = 0; j < 14; ++j) glds16(off, wo + cg * 256 + (size_t)j * 32 * XROW, lds0 + j * 8192 + wid * 1024); } }
    const int erow = tid >> 4, ecol = cg * 128 + (tid & 15) * 8, tok = qtile * 32 + erow; const bool live = ecol < D_;
    u32x4 xv = {}; if (live) xv = *(const u32x4*)(x + (size_t)tok * XROW + ecol * 2);
    const float g = ((const float*)(ws + WS_G))[tok];
    unsigned sel[8];
#pragma unroll
    for (int c = 0; c < 8; ++c) sel[c] = __builtin_amdgcn_readfirstlane(((const unsigned*)(ws + WS_SEL))[c]);
    float y[8] = {};
    constexpr int L3_Y = L3_O + 32768;
#pragma unroll
    for (int c = 0; c < 8; ++c) { const int t = 0xffff - (int)(sel[c] & 0xffff);
        if ((t >> 5) == qtile) {
            f32x4 a = {}; const float* pp = (const float*)(ws + WS_PART) + c * D_ + cg * 128 + r * 4;
            if (cg * 128 + r * 4 < D_) {
#pragma unroll
                for (int j = 0; j < 4; ++j) { const int mb = wid * 7 + 2 * j + hh; if (2 * j + hh < 7) a += *(const f32x4*)(pp + (size_t)mb * 8 * D_); } }
            LBAR();
            *(LAS f32x4*)(lds + L3_Y + (wid * 2 + hh) * 512 + r * 16) = a;
            LBAR();
            if (tok == t) { float s[8] = {};
#pragma unroll
                for (int j = 0; j < 16; ++j) { const f32x4 u = *(const LAS f32x4*)(lds + L3_Y + j * 512 + (tid & 15) * 32), v = *(const LAS f32x4*)(lds + L3_Y + j * 512 + (tid & 15) * 32 + 16);
#pragma unroll
                    for (int e = 0; e < 4; ++e) { s[e] += u[e]; s[4 + e] += v[e]; } }
                const unsigned o16 = sel[c] >> 16, u16 = (o16 & 0x8000u) ? (o16 & 0x7fffu) : (~o16 & 0xffffu);
                const float rs = rbf(1.f / (1.f + __expf(-bflo(u16))));
#pragma unroll
                for (int e = 0; e < 8; ++e) y[e] = rbf(rbf(s[e]) * rs); }
        } }
    WAIT_BAR(0);
    STAMP(ws, 7);
    const int tq = (lane & 15) >> 2, tp = lane & 3, tg = (lane >> 4) & 1;
    f32x16 acc = {};
    if (cg < 3 || ct < 2) {
        const int ch = (ct * 4 + tg * 2 + (tp >> 1)) ^ (4 * tq);
        const lptr wa = lds + (kh * 224 + 4 * hh + tq) * 256 + ch * 16 + (tp & 1) * 8, wz = wa - hh * 1024;
        const lptr ob = lds + L3_O + kh * 16384 + lane * 16;
#pragma unroll
        for (int j = 0; j < 8; ++j) {
            acc = MFMA32(ldtr2(wa + (28 * j) * 256, wa + (28 * j + 8) * 256), ldsr(ob + j * 2048), acc);
            acc = MFMA32(ldtr2(wa + (28 * j + 16) * 256, wz + (28 * j + 24) * 256), ldsr(ob + j * 2048 + 1024), acc);
        }
    }
    LBAR();
#pragma unroll
    for (int a = 0; a < 4; ++a) *(LAS f32x4*)(lds + kh * 16896 + r * 528 + (ct * 32 + 8 * a + 4 * hh) * 4) = (f32x4){acc[4 * a], acc[4 * a + 1], acc[4 * a + 2], acc[4 * a + 3]};
    LBAR();
    if (live) {
        const lptr pr = lds + erow * 528 + (tid & 15) * 32;
        const f32x4 a0 = *(const LAS f32x4*)pr + *(const LAS f32x4*)(pr + 16896), a1 = *(const LAS f32x4*)(pr + 16) + *(const LAS f32x4*)(pr + 16896 + 16);
        const float og = rbf(1.f - g); float res[8];
#pragma unroll
        for (int e = 0; e < 8; ++e) { const float xe = (e & 1) ? bfhi(xv[e >> 1]) : bflo(xv[e >> 1]); const float ae = e < 4 ? a0[e & 3] : a1[e & 3]; res[e] = g * ae + og * (xe + y[e]); }
        *(u32x4*)(out + (size_t)tok * XROW + ecol * 2) = (u32x4){cvtpk(res[0], res[1]), cvtpk(res[2], res[3]), cvtpk(res[4], res[5]), cvtpk(res[6], res[7])};
    }
    STAMP(ws, 8);
}

extern "C" void kernel_launch(void* const* d_in, const int* in_sizes, int n_in, void* d_out, int out_size, void* d_ws, size_t ws_size, hipStream_t stream) {
    const char* x = (const char*)d_in[0]; const char* wqkv = (const char*)d_in[1]; const char* wo = (const char*)d_in[2];
    const char* wr = (const char*)d_in[4]; const char* wg = (const char*)d_in[5]; const char* wu = (const char*)d_in[6]; const char* wd = (const char*)d_in[7]; const char* gw = (const char*)d_in[8];
    char* ws = (char*)d_ws;
    (void)hipFuncSetAttribute((const void*)k_qkv, hipFuncAttributeMaxDynamicSharedMemorySize, L1_BYTES);
    (void)hipFuncSetAttribute((const void*)k_attn, hipFuncAttributeMaxDynamicSharedMemorySize, L2_BYTES);
    (void)hipFuncSetAttribute((const void*)k_out, hipFuncAttributeMaxDynamicSharedMemorySize, L3_BYTES);
    k_qkv<<<256, 512, L1_BYTES, stream>>>(x, wqkv, wr, gw, ws);
    k_attn<<<256 + NMOD, 512, L2_BYTES, stream>>>(x, wg, wu, wd, ws);
    k_out<<<256, 512, L3_BYTES, stream>>>(x, wo, ws, (char*)d_out);
}
```

```cpp
#define PFM 0
#include <hip/hip_runtime.h>
#include <hip/hip_bf16.h>
#include <stdint.h>

typedef short bf16x8 __attribute__((ext_vector_type(8)));
typedef short s16x4 __attribute__((ext_vector_type(4)));
typedef float f32x16 __attribute__((ext_vector_type(16)));
typedef float f32x4 __attribute__((ext_vector_type(4)));
typedef float f32x2 __attribute__((ext_vector_type(2)));
typedef unsigned u32x4 __attribute__((ext_vector_type(4)));
typedef unsigned u32x2 __attribute__((ext_vector_type(2)));
typedef __bf16 bf16x2_t __attribute__((ext_vector_type(2)));
typedef unsigned long long u64;
typedef __amdgpu_buffer_rsrc_t rsrc_t;
#define LAS __attribute__((address_space(3)))
#define GAS __attribute__((address_space(1)))
typedef LAS char* lptr;
#define MFMA32(a, b, c) __builtin_amdgcn_mfma_f32_32x32x16_bf16(a, b, c, 0, 0, 0)
#define MFMA16(a, b, c) __builtin_amdgcn_mfma_f32_16x16x32_bf16(a, b, c, 0, 0, 0)
#define SBAR() __builtin_amdgcn_sched_barrier(0)
#define PIN(x) asm volatile("" : "+v"(x))
#define DEVI __device__ __forceinline__
#define RFL(x) __builtin_amdgcn_readfirstlane(x)
#define BC8(p) __builtin_bit_cast(bf16x8, p)
#define EX(v) __builtin_amdgcn_exp2f(v)

constexpr int XROW = 896, WROW = 2688, FROW = 3584;
constexpr int HR = 6, HI = 9;
constexpr float QSCALE = 0.18898223650461362f * 1.4426950408889634f;
constexpr unsigned WS_KV = 0, WS_O = 4u << 20, WS_G = 6u << 20, WS_CAND = WS_G + 16384, WS_SEL = WS_CAND + 4096, WS_P = WS_G + 65536, WS_Y = WS_P + 112 * 4096;
constexpr unsigned WS_FQ0 = 7u << 20, WS_FB = WS_FQ0 + 32768, WS_FA = WS_FB + 32768, WS_FO = WS_FA + 32768, WS_FH = WS_FO + 131072, WS_FY = WS_FH + 16384, WS_EP = WS_FY + 16384, WS_BYTES = 8u << 20;
#define FS 128
#define MODL 2
#define WOS 2
#define PFAT M_CB
#define PFN 8
#define S2S 3
#define PFS 8
#define WOD 16
#define GATE M_RS
#define GATEN 1
#define PD 10
constexpr int L_W = 0, L_WQ = 114688, L_QI = 143360, L_PP = 151552, L_MOD = 110592, L_ML = 100352, L_Y = 104448, L_CB = 110592, L_MISC = 159744, L_BYTES = 163840;
constexpr int M_CQ0 = 0, M_LV = 4, M_CW = 8, M_CQ = 12, M_CKEY = 16, M_COUT = 20, M_P = 24, M_XI = 140, M_RS = 144, M_CC = 28, M_X1 = 32, M_X2 = 36, M_X3 = 40, M_WQ = 44, M_LAND = 48, M_Q0S = 52, M_WO = 56, M_AUX = 60,
              M_OL = 64, M_TAGS = 68, M_TAG = 72, M_CQI = 80, M_KVD = 92, M_CA = 84, M_CB = 88, M_F = 96, M_CP = 128, M_CO = 160, M_SEL = 192, M_PROG = 512, M_KEYS = 1024, M_JUNK = 2048;
DEVI int xq(int q) { return ((q + 2) & 3) * 28672; }

#define STAMP(k)
#define STAMP_DECL
#define STAMP_FLUSH()
#define STAMP_ARG
#define STAMP_PASS

DEVI unsigned cvtpk(float lo, float hi) { const f32x2 v = {lo, hi}; return __builtin_bit_cast(unsigned, __builtin_convertvector(v, bf16x2_t)); }
DEVI float bflo(unsigned u) { return __uint_as_float(u << 16); }
DEVI float bfhi(unsigned u) { return __uint_as_float(u & 0xffff0000u); }
DEVI float rbf(float f) { return bflo(cvtpk(f, f)); }
DEVI float sigm(float v) { return __builtin_amdgcn_rcpf(1.f + __expf(-v)); }
DEVI rsrc_t mk(const void* p, unsigned bytes) { return __builtin_amdgcn_make_buffer_rsrc((void*)p, 0, bytes, 0x00020000); }
template <int N, int LS, int SS, bool SC1> DEVI void dma(unsigned lds, unsigned voff, rsrc_t rs, unsigned soff) {
    unsigned t;
    if (SC1) asm volatile("s_mov_b32 m0, %1\n\ts_mov_b32 %0, %4\n\t.rept %c5\n\tbuffer_load_dwordx4 %2, %3, %0 offen sc1 lds\n\ts_add_u32 m0, m0, %6\n\ts_add_u32 %0, %0, %7\n\t.endr"
                          : "=&s"(t) : "s"(lds), "v"(voff), "s"(rs), "s"(soff), "i"(N), "s"(LS), "s"(SS) : "memory", "scc");
    else asm volatile("s_mov_b32 m0, %1\n\ts_mov_b32 %0, %4\n\t.rept %c5\n\tbuffer_load_dwordx4 %2, %3, %0 offen lds\n\ts_add_u32 m0, m0, %6\n\ts_add_u32 %0, %0, %7\n\t.endr"
                      : "=&s"(t) : "s"(lds), "v"(voff), "s"(rs), "s"(soff), "i"(N), "s"(LS), "s"(SS) : "memory", "scc");
}
template <int N> DEVI void dma2(unsigned lds, unsigned LS, unsigned va, unsigned vb, rsrc_t rs, unsigned soff, unsigned SS) {
    unsigned t;
    asm volatile("s_mov_b32 m0, %1\n\ts_mov_b32 %0, %5\n\t.rept %c6\n\tbuffer_load_dwordx4 %2, %4, %0 offen lds\n\ts_add_u32 m0, m0, %7\n\ts_add_u32 %0, %0, %8\n\t"
                 "buffer_load_dwordx4 %3, %4, %0 offen lds\n\ts_add_u32 m0, m0, %7\n\ts_add_u32 %0, %0, %8\n\t.endr"
                 : "=&s"(t) : "s"(lds), "v"(va), "v"(vb), "s"(rs), "s"(soff), "i"(N), "s"(LS), "s"(SS) : "memory", "scc");
}
DEVI void dma1(unsigned lds, unsigned voff, rsrc_t rs, unsigned soff) {
    asm volatile("s_mov_b32 m0, %0\n\ts_nop 0\n\tbuffer_load_dwordx4 %1, %2, %3 offen lds" :: "s"(lds), "v"(voff), "s"(rs), "s"(soff) : "memory");
}
DEVI bf16x8 ldsr(lptr p) { return *(const LAS bf16x8*)p; }
DEVI s16x4 ldtr(lptr p) { return __builtin_amdgcn_ds_read_tr16_b64_v4i16((LAS s16x4*)p); }
DEVI bf16x8 ldtr2(lptr p0, lptr p1) { s16x4 a = ldtr(p0), b = ldtr(p1); return (bf16x8){a[0], a[1], a[2], a[3], b[0], b[1], b[2], b[3]}; }
#define WAITV(N) asm volatile("s_waitcnt vmcnt(" #N ")" ::: "memory")
#define WAITL() asm volatile("s_waitcnt lgkmcnt(0)" ::: "memory")
#define BAR() asm volatile("s_barrier" ::: "memory")
#define WAIT_BAR(N) asm volatile("s_waitcnt vmcnt(" #N ") lgkmcnt(0)\n\ts_barrier" ::: "memory")
#define LBAR() asm volatile("s_waitcnt lgkmcnt(0)\n\ts_barrier" ::: "memory")
DEVI void swap32(unsigned& a, unsigned& b) { auto r = __builtin_amdgcn_permlane32_swap(a, b, false, false); a = r[0]; b = r[1]; }
DEVI void swap16(unsigned& a, unsigned& b) { auto r = __builtin_amdgcn_permlane16_swap(a, b, false, false); a = r[0]; b = r[1]; }
template <int OFF> DEVI void lset(lptr p, unsigned v) { asm volatile("ds_write_b32 %0, %1 offset:%c2" :: "v"(p), "v"(v), "i"(OFF) : "memory"); }
template <int OFF> DEVI void ladd(lptr p, unsigned one) { asm volatile("s_mov_b64 exec, 1\n\tds_add_u32 %0, %1 offset:%c2\n\ts_mov_b64 exec, -1" :: "v"(p), "v"(one), "i"(OFF) : "memory"); }
template <int OFF> DEVI unsigned laddr(lptr p, unsigned one) { unsigned o, r;
    asm volatile("s_mov_b64 exec, 1\n\tds_add_rtn_u32 %0, %2, %3 offset:%c4\n\ts_waitcnt lgkmcnt(0)\n\tv_readfirstlane_b32 %1, %0\n\ts_mov_b64 exec, -1" : "=&v"(o), "=s"(r) : "v"(p), "v"(one), "i"(OFF) : "memory");
    return r; }
template <int OFF, int SLEEP = 0> DEVI void lwait(lptr p, unsigned need) {
    unsigned v, n;
    asm volatile("s_movk_i32 %1, 0x7fff\n1:\n\tds_read_b32 %0, %2 offset:%c5\n\ts_sub_u32 %1, %1, 1\n\ts_waitcnt lgkmcnt(0)\n\ts_cbranch_scc1 2f\n\tv_cmp_gt_u32 vcc, %3, %0\n\t"
                 ".if %c4\n\ts_cbranch_vccz 2f\n\ts_sleep %c4\n\ts_branch 1b\n\t.else\n\ts_cbranch_vccnz 1b\n\t.endif\n2:"
                 : "=&v"(v), "=&s"(n) : "v"(p), "s"(need), "i"(SLEEP), "i"(OFF) : "memory", "scc", "vcc");
}
DEVI void gset(char* ws, unsigned off, u64 v) {
    asm volatile("s_mov_b64 exec, 1\n\tglobal_store_dwordx2 %0, %1, %2 sc1\n\ts_mov_b64 exec, -1" :: "v"(off), "v"(v), "s"(ws) : "memory"); }
template <int SLEEP = 0> DEVI void gwait(const char* ws, unsigned off, u64 tag) {
    u64 v; unsigned n = 1u << 13;
    asm volatile("1:\n\tglobal_load_dwordx2 %0, %2, %3 sc1\n\ts_sub_u32 %1, %1, 1\n\ts_waitcnt vmcnt(0)\n\ts_cbranch_scc1 2f\n\tv_cmp_ne_u64 vcc, %4, %0\n\t.if %c5\n\ts_cbranch_vccz 2f\n\ts_sleep %c5\n\ts_branch 1b\n\t.else\n\ts_cbranch_vccnz 1b\n\t.endif\n2:"
                 : "=&v"(v), "+&s"(n) : "v"(off), "s"(ws), "s"(tag), "i"(SLEEP) : "memory", "scc", "vcc");
}
DEVI void st16(rsrc_t rs, unsigned off, u32x4 v) { __builtin_amdgcn_raw_buffer_store_b128(v, rs, off, 0, 16); }
DEVI u32x4 ld16(rsrc_t rs, unsigned off) { return __builtin_amdgcn_raw_buffer_load_b128(rs, off, 0, 16); }
DEVI u32x4 rec(u64 tag, unsigned key) { return (u32x4){(unsigned)tag, key, (unsigned)(tag >> 32), key}; }
DEVI bool rec_ok(u32x4 r, u64 tag) { return r[0] == (unsigned)tag && r[2] == (unsigned)(tag >> 32) && r[1] == r[3]; }
template <int SLEEP> DEVI unsigned rwait(rsrc_t rs, unsigned off, u64 tag) { u32x4 r;
#pragma unroll 1
    for (int i = 0; i < (1 << 13); ++i) { r = ld16(rs, off); if (__all(rec_ok(r, tag))) break; __builtin_amdgcn_s_sleep(SLEEP); }
    return r[1]; }
DEVI unsigned umax(unsigned a, unsigned b) { return a > b ? a : b; }
DEVI unsigned wave_umax(unsigned v) {
    v = umax(v, __builtin_amdgcn_update_dpp(0u, v, 0x111, 0xf, 0xf, false)); v = umax(v, __builtin_amdgcn_update_dpp(0u, v, 0x112, 0xf, 0xf, false));
    v = umax(v, __builtin_amdgcn_update_dpp(0u, v, 0x114, 0xf, 0xf, false)); v = umax(v, __builtin_amdgcn_update_dpp(0u, v, 0x118, 0xf, 0xf, false));
    v = umax(v, __builtin_amdgcn_update_dpp(0u, v, 0x142, 0xa, 0xf, false)); v = umax(v, __builtin_amdgcn_update_dpp(0u, v, 0x143, 0xc, 0xf, false));
    return __builtin_amdgcn_readlane(v, 63);
}
#define MX3(a, b, c) __builtin_fmaxf(__builtin_fmaxf((a), (b)), (c))
DEVI float max16(const f32x16& s) {
    float a = MX3(s[0], s[1], s[2]), b = MX3(s[3], s[4], s[5]); a = MX3(a, s[6], s[7]); b = MX3(b, s[8], s[9]); a = MX3(a, s[10], s[11]); b = MX3(b, s[12], s[13]); a = MX3(a, s[14], s[15]);
    return __builtin_fmaxf(a, b);
}
DEVI float halfmax(float m) { unsigned a = __float_as_uint(m), b = a; swap32(a, b); return __builtin_fmaxf(__uint_as_float(a), __uint_as_float(b)); }

DEVI u64 ltag(lptr vm) { lwait<M_TAGS>(vm, 1); const u32x2 v = *(volatile LAS u32x2*)(vm + M_TAG); return (u64)(unsigned)RFL(v[0]) | ((u64)(unsigned)RFL(v[1]) << 32); }
DEVI void touch(unsigned lds, unsigned voff, rsrc_t rs, unsigned soff) { asm volatile("s_mov_b32 m0, %0\n\ts_nop 0\n\tbuffer_load_dword %1, %2, %3 offen lds" :: "s"(lds), "v"(voff), "s"(rs), "s"(soff) : "memory"); }
DEVI void ct_to_chunks(const f32x16& c, float sc, u32x4& p0, u32x4& p1) {
    unsigned a0 = cvtpk(c[0] * sc, c[1] * sc), a1 = cvtpk(c[2] * sc, c[3] * sc), b0 = cvtpk(c[4] * sc, c[5] * sc), b1 = cvtpk(c[6] * sc, c[7] * sc);
    unsigned c0 = cvtpk(c[8] * sc, c[9] * sc), c1 = cvtpk(c[10] * sc, c[11] * sc), d0 = cvtpk(c[12] * sc, c[13] * sc), d1 = cvtpk(c[14] * sc, c[15] * sc);
    swap32(a0, b0); swap32(a1, b1); swap32(c0, d0); swap32(c1, d1);
    p0 = (u32x4){a0, a1, b0, b1}; p1 = (u32x4){c0, c1, d0, d1};
}
#define ATTN_LOOP \
    "v_mfma_f32_32x32x16_bf16 v[16:31], v[80:83], v[72:75], 0\n\ts_mov_b32 %[s1], 196608\n\tv_exp_f32 v0, v0\n\tv_exp_f32 v1, v1\n\t" \
    "v_exp_f32 v2, v2\n\tv_exp_f32 v3, v3\n\tv_mfma_f32_32x32x16_bf16 v[16:31], v[84:87], v[76:79], v[16:31]\n\tv_exp_f32 v4, v4\n\t" \
    "v_exp_f32 v5, v5\n\tv_cvt_pk_bf16_f32 v112, v0, v1\n\tv_exp_f32 v6, v6\n\tv_exp_f32 v7, v7\n\t" \
    "v_cvt_pk_bf16_f32 v113, v2, v3\n\ts_waitcnt vmcnt(4)\n\tv_exp_f32 v8, v8\n\tv_exp_f32 v9, v9\n\t" \
    "v_cvt_pk_bf16_f32 v114, v4, v5\n\tv_exp_f32 v10, v10\n\tv_exp_f32 v11, v11\n\tv_cvt_pk_bf16_f32 v115, v6, v7\n\t" \
    "v_exp_f32 v128, v12\n\tv_exp_f32 v129, v13\n\tv_cvt_pk_bf16_f32 v120, v8, v9\n\tv_mfma_f32_32x32x16_bf16 v[32:47], v[96:99], v[112:115], 0\n\t" \
    "buffer_load_dwordx4 v[140:143], %[vo], %[rs], %[s0] offen offset:2048 sc1\n\tv_exp_f32 v130, v14\n\tv_exp_f32 v131, v15\n\tv_cvt_pk_bf16_f32 v121, v10, v11\n\t" \
    "v_mfma_f32_32x32x16_bf16 v[0:15], v[88:91], v[64:67], 0\n\tbuffer_load_dwordx4 v[144:147], %[vo], %[rs], %[s0] offen offset:3072 sc1\n\tv_exp_f32 v16, v16\n\tv_exp_f32 v17, v17\n\t" \
    "v_cvt_pk_bf16_f32 v122, v128, v129\n\tv_exp_f32 v18, v18\n\tv_exp_f32 v19, v19\n\tv_cvt_pk_bf16_f32 v123, v130, v131\n\t" \
    "v_mfma_f32_32x32x16_bf16 v[0:15], v[92:95], v[68:71], v[0:15]\n\tbuffer_load_dwordx4 v[80:83], %[vo], %[rs], %[s1] offen sc1\n\tv_exp_f32 v20, v20\n\tv_exp_f32 v21, v21\n\t" \
    "v_cvt_pk_bf16_f32 v116, v16, v17\n\tv_exp_f32 v22, v22\n\tv_exp_f32 v23, v23\n\tv_cvt_pk_bf16_f32 v117, v18, v19\n\t" \
    "v_mfma_f32_32x32x16_bf16 v[32:47], v[100:103], v[120:123], v[32:47]\n\tbuffer_load_dwordx4 v[84:87], %[vo], %[rs], %[s1] offen offset:1024 sc1\n\tv_exp_f32 v24, v24\n\tv_exp_f32 v25, v25\n\t" \
    "v_cvt_pk_bf16_f32 v118, v20, v21\n\tv_exp_f32 v26, v26\n\tv_exp_f32 v27, v27\n\tv_cvt_pk_bf16_f32 v119, v22, v23\n\t" \
    "v_exp_f32 v128, v28\n\tv_exp_f32 v129, v29\n\tv_cvt_pk_bf16_f32 v124, v24, v25\n\tv_mfma_f32_32x32x16_bf16 v[48:63], v[96:99], v[116:119], 0\n\t" \
    "v_exp_f32 v130, v30\n\tv_exp_f32 v131, v31\n\tv_cvt_pk_bf16_f32 v125, v26, v27\n\tv_mfma_f32_32x32x16_bf16 v[16:31], v[88:91], v[72:75], 0\n\t" \
    "s_mov_b32 %[s0], 4096\n\tds_read_b32 v148, %[ld]\n\tv_exp_f32 v0, v0\n\tv_exp_f32 v1, v1\n\t" \
    "v_cvt_pk_bf16_f32 v126, v128, v129\n\tv_exp_f32 v2, v2\n\tv_exp_f32 v3, v3\n\tv_cvt_pk_bf16_f32 v127, v130, v131\n\t" \
    "v_mfma_f32_32x32x16_bf16 v[16:31], v[92:95], v[76:79], v[16:31]\n\tv_exp_f32 v4, v4\n\tv_exp_f32 v5, v5\n\tv_cvt_pk_bf16_f32 v112, v0, v1\n\t" \
    "v_exp_f32 v6, v6\n\tv_exp_f32 v7, v7\n\tv_cvt_pk_bf16_f32 v113, v2, v3\n\tv_mfma_f32_32x32x16_bf16 v[48:63], v[100:103], v[124:127], v[48:63]\n\t" \
    "s_waitcnt vmcnt(4) lgkmcnt(0)\n\tv_exp_f32 v8, v8\n\tv_exp_f32 v9, v9\n\tv_cvt_pk_bf16_f32 v114, v4, v5\n\t" \
    "v_exp_f32 v10, v10\n\tv_exp_f32 v11, v11\n\tv_cvt_pk_bf16_f32 v115, v6, v7\n\tv_exp_f32 v128, v12\n\t" \
    "v_exp_f32 v129, v13\n\tv_cvt_pk_bf16_f32 v120, v8, v9\n\tv_mfma_f32_32x32x16_bf16 v[32:47], v[104:107], v[112:115], v[32:47]\n\tbuffer_load_dwordx4 v[96:99], %[vo], %[rs], %[s1] offen offset:2048 sc1\n\t" \
    "v_exp_f32 v130, v14\n\tv_exp_f32 v131, v15\n\tv_cvt_pk_bf16_f32 v121, v10, v11\n\tv_mfma_f32_32x32x16_bf16 v[0:15], v[132:135], v[64:67], 0\n\t" \
    "buffer_load_dwordx4 v[100:103], %[vo], %[rs], %[s1] offen offset:3072 sc1\n\tv_cmp_gt_u32 vcc, 2, v148\n\tv_exp_f32 v16, v16\n\tv_exp_f32 v17, v17\n\t" \
    "v_cvt_pk_bf16_f32 v122, v128, v129\n\tv_exp_f32 v18, v18\n\tv_exp_f32 v19, v19\n\tv_cvt_pk_bf16_f32 v123, v130, v131\n\t" \
    "v_mfma_f32_32x32x16_bf16 v[0:15], v[136:139], v[68:71], v[0:15]\n\ts_cbranch_vccnz .Ls1_%=\n\t.Lb1_%=:\n\tbuffer_load_dwordx4 v[88:91], %[vo], %[rs], %[s0] offen sc1\n\t" \
    "v_exp_f32 v20, v20\n\tv_exp_f32 v21, v21\n\tv_cvt_pk_bf16_f32 v116, v16, v17\n\tv_exp_f32 v22, v22\n\t" \
    "v_exp_f32 v23, v23\n\tv_cvt_pk_bf16_f32 v117, v18, v19\n\tv_mfma_f32_32x32x16_bf16 v[32:47], v[108:111], v[120:123], v[32:47]\n\tbuffer_load_dwordx4 v[92:95], %[vo], %[rs], %[s0] offen offset:1024 sc1\n\t" \
    "v_exp_f32 v24, v24\n\tv_exp_f32 v25, v25\n\tv_cvt_pk_bf16_f32 v118, v20, v21\n\tv_exp_f32 v26, v26\n\t" \
    "v_exp_f32 v27, v27\n\tv_cvt_pk_bf16_f32 v119, v22, v23\n\tv_exp_f32 v128, v28\n\tv_exp_f32 v129, v29\n\t" \
    "v_cvt_pk_bf16_f32 v124, v24, v25\n\tv_mfma_f32_32x32x16_bf16 v[48:63], v[104:107], v[116:119], v[48:63]\n\tv_exp_f32 v130, v30\n\tv_exp_f32 v131, v31\n\t" \
    "v_cvt_pk_bf16_f32 v125, v26, v27\n\tv_mfma_f32_32x32x16_bf16 v[16:31], v[132:135], v[72:75], 0\n\ts_mov_b32 %[s1], 69632\n\tv_exp_f32 v0, v0\n\t" \
    "v_exp_f32 v1, v1\n\tv_cvt_pk_bf16_f32 v126, v128, v129\n\tv_exp_f32 v2, v2\n\tv_exp_f32 v3, v3\n\t" \
    "v_cvt_pk_bf16_f32 v127, v130, v131\n\tv_mfma_f32_32x32x16_bf16 v[16:31], v[136:139], v[76:79], v[16:31]\n\tv_exp_f32 v4, v4\n\tv_exp_f32 v5, v5\n\t" \
    "v_cvt_pk_bf16_f32 v112, v0, v1\n\tv_exp_f32 v6, v6\n\tv_exp_f32 v7, v7\n\tv_cvt_pk_bf16_f32 v113, v2, v3\n\t" \
    "v_mfma_f32_32x32x16_bf16 v[48:63], v[108:111], v[124:127], v[48:63]\n\ts_waitcnt vmcnt(4)\n\tv_exp_f32 v8, v8\n\tv_exp_f32 v9, v9\n\t" \
    "v_cvt_pk_bf16_f32 v114, v4, v5\n\tv_exp_f32 v10, v10\n\tv_exp_f32 v11, v11\n\tv_cvt_pk_bf16_f32 v115, v6, v7\n\t" \
    "v_exp_f32 v128, v12\n\tv_exp_f32 v129, v13\n\tv_cvt_pk_bf16_f32 v120, v8, v9\n\tv_mfma_f32_32x32x16_bf16 v[32:47], v[140:143], v[112:115], v[32:47]\n\t" \
    "buffer_load_dwordx4 v[104:107], %[vo], %[rs], %[s0] offen offset:2048 sc1\n\tv_exp_f32 v130, v14\n\tv_exp_f32 v131, v15\n\tv_cvt_pk_bf16_f32 v121, v10, v11\n\t" \
    "v_mfma_f32_32x32x16_bf16 v[0:15], v[80:83], v[64:67], 0\n\tbuffer_load_dwordx4 v[108:111], %[vo], %[rs], %[s0] offen offset:3072 sc1\n\tv_exp_f32 v16, v16\n\tv_exp_f32 v17, v17\n\t" \
    "v_cvt_pk_bf16_f32 v122, v128, v129\n\tv_exp_f32 v18, v18\n\tv_exp_f32 v19, v19\n\tv_cvt_pk_bf16_f32 v123, v130, v131\n\t" \
    "v_mfma_f32_32x32x16_bf16 v[0:15], v[84:87], v[68:71], v[0:15]\n\tbuffer_load_dwordx4 v[132:135], %[vo], %[rs], %[s1] offen sc1\n\tv_exp_f32 v20, v20\n\tv_exp_f32 v21, v21\n\t" \
    "v_cvt_pk_bf16_f32 v116, v16, v17\n\tv_exp_f32 v22, v22\n\tv_exp_f32 v23, v23\n\tv_cvt_pk_bf16_f32 v117, v18, v19\n\t" \
    "v_mfma_f32_32x32x16_bf16 v[32:47], v[144:147], v[120:123], v[32:47]\n\tbuffer_load_dwordx4 v[136:139], %[vo], %[rs], %[s1] offen offset:1024 sc1\n\tv_exp_f32 v24, v24\n\tv_exp_f32 v25, v25\n\t" \
    "v_cvt_pk_bf16_f32 v118, v20, v21\n\tv_exp_f32 v26, v26\n\tv_exp_f32 v27, v27\n\tv_cvt_pk_bf16_f32 v119, v22, v23\n\t" \
    "v_exp_f32 v128, v28\n\tv_exp_f32 v129, v29\n\tv_cvt_pk_bf16_f32 v124, v24, v25\n\tv_mfma_f32_32x32x16_bf16 v[48:63], v[140:143], v[116:119], v[48:63]\n\t" \
    "v_exp_f32 v130, v30\n\tv_exp_f32 v131, v31\n\tv_cvt_pk_bf16_f32 v125, v26, v27\n\tv_mfma_f32_32x32x16_bf16 v[16:31], v[80:83], v[72:75], 0\n\t" \
    "s_mov_b32 %[s0], 135168\n\tv_exp_f32 v0, v0\n\tv_exp_f32 v1, v1\n\tv_cvt_pk_bf16_f32 v126, v128, v129\n\t" \
    "v_exp_f32 v2, v2\n\tv_exp_f32 v3, v3\n\tv_cvt_pk_bf16_f32 v127, v130, v131\n\tv_mfma_f32_32x32x16_bf16 v[16:31], v[84:87], v[76:79], v[16:31]\n\t" \
    "v_exp_f32 v4, v4\n\tv_exp_f32 v5, v5\n\tv_cvt_pk_bf16_f32 v112, v0, v1\n\tv_exp_f32 v6, v6\n\t" \
    "v_exp_f32 v7, v7\n\tv_cvt_pk_bf16_f32 v113, v2, v3\n\tv_mfma_f32_32x32x16_bf16 v[48:63], v[144:147], v[124:127], v[48:63]\n\ts_waitcnt vmcnt(4)\n\t" \
    "v_exp_f32 v8, v8\n\tv_exp_f32 v9, v9\n\tv_cvt_pk_bf16_f32 v114, v4, v5\n\tv_exp_f32 v10, v10\n\t" \
    "v_exp_f32 v11, v11\n\tv_cvt_pk_bf16_f32 v115, v6, v7\n\tv_exp_f32 v128, v12\n\tv_exp_f32 v129, v13\n\t" \
    "v_cvt_pk_bf16_f32 v120, v8, v9\n\tv_mfma_f32_32x32x16_bf16 v[32:47], v[96:99], v[112:115], v[32:47]\n\tbuffer_load_dwordx4 v[140:143], %[vo], %[rs], %[s1] offen offset:2048 sc1\n\tv_exp_f32 v130, v14\n\t" \
    "v_exp_f32 v131, v15\n\tv_cvt_pk_bf16_f32 v121, v10, v11\n\tv_mfma_f32_32x32x16_bf16 v[0:15], v[88:91], v[64:67], 0\n\tbuffer_load_dwordx4 v[144:147], %[vo], %[rs], %[s1] offen offset:3072 sc1\n\t" \
    "v_exp_f32 v16, v16\n\tv_exp_f32 v17, v17\n\tv_cvt_pk_bf16_f32 v122, v128, v129\n\tv_exp_f32 v18, v18\n\t" \
    "v_exp_f32 v19, v19\n\tv_cvt_pk_bf16_f32 v123, v130, v131\n\tv_mfma_f32_32x32x16_bf16 v[0:15], v[92:95], v[68:71], v[0:15]\n\tbuffer_load_dwordx4 v[80:83], %[vo], %[rs], %[s0] offen sc1\n\t" \
    "v_exp_f32 v20, v20\n\tv_exp_f32 v21, v21\n\tv_cvt_pk_bf16_f32 v116, v16, v17\n\tv_exp_f32 v22, v22\n\t" \
    "v_exp_f32 v23, v23\n\tv_cvt_pk_bf16_f32 v117, v18, v19\n\tv_mfma_f32_32x32x16_bf16 v[32:47], v[100:103], v[120:123], v[32:47]\n\tbuffer_load_dwordx4 v[84:87], %[vo], %[rs], %[s0] offen offset:1024 sc1\n\t" \
    "v_exp_f32 v24, v24\n\tv_exp_f32 v25, v25\n\tv_cvt_pk_bf16_f32 v118, v20, v21\n\tv_exp_f32 v26, v26\n\t" \
    "v_exp_f32 v27, v27\n\tv_cvt_pk_bf16_f32 v119, v22, v23\n\tv_exp_f32 v128, v28\n\tv_exp_f32 v129, v29\n\t" \
    "v_cvt_pk_bf16_f32 v124, v24, v25\n\tv_mfma_f32_32x32x16_bf16 v[48:63], v[96:99], v[116:119], v[48:63]\n\tv_exp_f32 v130, v30\n\tv_exp_f32 v131, v31\n\t" \
    "v_cvt_pk_bf16_f32 v125, v26, v27\n\tv_mfma_f32_32x32x16_bf16 v[16:31], v[88:91], v[72:75], 0\n\ts_mov_b32 %[s1], 200704\n\tv_exp_f32 v0, v0\n\t" \
    "v_exp_f32 v1, v1\n\tv_cvt_pk_bf16_f32 v126, v128, v129\n\tv_exp_f32 v2, v2\n\tv_exp_f32 v3, v3\n\t" \
    "v_cvt_pk_bf16_f32 v127, v130, v131\n\tv_mfma_f32_32x32x16_bf16 v[16:31], v[92:95], v[76:79], v[16:31]\n\tv_exp_f32 v4, v4\n\tv_exp_f32 v5, v5\n\t" \
    "v_cvt_pk_bf16_f32 v112, v0, v1\n\tv_exp_f32 v6, v6\n\tv_exp_f32 v7, v7\n\tv_cvt_pk_bf16_f32 v113, v2, v3\n\t" \
    "v_mfma_f32_32x32x16_bf16 v[48:63], v[100:103], v[124:127], v[48:63]\n\ts_waitcnt vmcnt(4)\n\tv_exp_f32 v8, v8\n\tv_exp_f32 v9, v9\n\t" \
    "v_cvt_pk_bf16_f32 v114, v4, v5\n\tv_exp_f32 v10, v10\n\tv_exp_f32 v11, v11\n\tv_cvt_pk_bf16_f32 v115, v6, v7\n\t" \
    "v_exp_f32 v128, v12\n\tv_exp_f32 v129, v13\n\tv_cvt_pk_bf16_f32 v120, v8, v9\n\tv_mfma_f32_32x32x16_bf16 v[32:47], v[104:107], v[112:115], v[32:47]\n\t" \
    "buffer_load_dwordx4 v[96:99], %[vo], %[rs], %[s0] offen offset:2048 sc1\n\tv_exp_f32 v130, v14\n\tv_exp_f32 v131, v15\n\tv_cvt_pk_bf16_f32 v121, v10, v11\n\t" \
    "v_mfma_f32_32x32x16_bf16 v[0:15], v[132:135], v[64:67], 0\n\tbuffer_load_dwordx4 v[100:103], %[vo], %[rs], %[s0] offen offset:3072 sc1\n\tv_exp_f32 v16, v16\n\tv_exp_f32 v17, v17\n\t" \
    "v_cvt_pk_bf16_f32 v122, v128, v129\n\tv_exp_f32 v18, v18\n\tv_exp_f32 v19, v19\n\tv_cvt_pk_bf16_f32 v123, v130, v131\n\t" \
    "v_mfma_f32_32x32x16_bf16 v[0:15], v[136:139], v[68:71], v[0:15]\n\tbuffer_load_dwordx4 v[88:91], %[vo], %[rs], %[s1] offen sc1\n\tv_exp_f32 v20, v20\n\tv_exp_f32 v21, v21\n\t" \
    "v_cvt_pk_bf16_f32 v116, v16, v17\n\tv_exp_f32 v22, v22\n\tv_exp_f32 v23, v23\n\tv_cvt_pk_bf16_f32 v117, v18, v19\n\t" \
    "v_mfma_f32_32x32x16_bf16 v[32:47], v[108:111], v[120:123], v[32:47]\n\tbuffer_load_dwordx4 v[92:95], %[vo], %[rs], %[s1] offen offset:1024 sc1\n\tv_exp_f32 v24, v24\n\tv_exp_f32 v25, v25\n\t" \
    "v_cvt_pk_bf16_f32 v118, v20, v21\n\tv_exp_f32 v26, v26\n\tv_exp_f32 v27, v27\n\tv_cvt_pk_bf16_f32 v119, v22, v23\n\t" \
    "v_exp_f32 v128, v28\n\tv_exp_f32 v129, v29\n\tv_cvt_pk_bf16_f32 v124, v24, v25\n\tv_mfma_f32_32x32x16_bf16 v[48:63], v[104:107], v[116:119], v[48:63]\n\t" \
    "v_exp_f32 v130, v30\n\tv_exp_f32 v131, v31\n\tv_cvt_pk_bf16_f32 v125, v26, v27\n\tv_mfma_f32_32x32x16_bf16 v[16:31], v[132:135], v[72:75], 0\n\t" \
    "s_mov_b32 %[s0], 8192\n\tv_exp_f32 v0, v0\n\tv_exp_f32 v1, v1\n\tv_cvt_pk_bf16_f32 v126, v128, v129\n\t" \
    "v_exp_f32 v2, v2\n\tv_exp_f32 v3, v3\n\tv_cvt_pk_bf16_f32 v127, v130, v131\n\tv_mfma_f32_32x32x16_bf16 v[16:31], v[136:139], v[76:79], v[16:31]\n\t" \
    "v_exp_f32 v4, v4\n\tv_exp_f32 v5, v5\n\tv_cvt_pk_bf16_f32 v112, v0, v1\n\tv_exp_f32 v6, v6\n\t" \
    "v_exp_f32 v7, v7\n\tv_cvt_pk_bf16_f32 v113, v2, v3\n\tv_mfma_f32_32x32x16_bf16 v[48:63], v[108:111], v[124:127], v[48:63]\n\ts_waitcnt vmcnt(4)\n\t" \
    "v_exp_f32 v8, v8\n\tv_exp_f32 v9, v9\n\tv_cvt_pk_bf16_f32 v114, v4, v5\n\tv_exp_f32 v10, v10\n\t" \
    "v_exp_f32 v11, v11\n\tv_cvt_pk_bf16_f32 v115, v6, v7\n\tv_exp_f32 v128, v12\n\tv_exp_f32 v129, v13\n\t" \
    "v_cvt_pk_bf16_f32 v120, v8, v9\n\tv_mfma_f32_32x32x16_bf16 v[32:47], v[140:143], v[112:115], v[32:47]\n\tbuffer_load_dwordx4 v[104:107], %[vo], %[rs], %[s1] offen offset:2048 sc1\n\tv_exp_f32 v130, v14\n\t" \
    "v_exp_f32 v131, v15\n\tv_cvt_pk_bf16_f32 v121, v10, v11\n\tv_mfma_f32_32x32x16_bf16 v[0:15], v[80:83], v[64:67], 0\n\tbuffer_load_dwordx4 v[108:111], %[vo], %[rs], %[s1] offen offset:3072 sc1\n\t" \
    "v_exp_f32 v16, v16\n\tv_exp_f32 v17, v17\n\tv_cvt_pk_bf16_f32 v122, v128, v129\n\tv_exp_f32 v18, v18\n\t" \
    "v_exp_f32 v19, v19\n\tv_cvt_pk_bf16_f32 v123, v130, v131\n\tv_mfma_f32_32x32x16_bf16 v[0:15], v[84:87], v[68:71], v[0:15]\n\tbuffer_load_dwordx4 v[132:135], %[vo], %[rs], %[s0] offen sc1\n\t" \
    "v_exp_f32 v20, v20\n\tv_exp_f32 v21, v21\n\tv_cvt_pk_bf16_f32 v116, v16, v17\n\tv_exp_f32 v22, v22\n\t" \
    "v_exp_f32 v23, v23\n\tv_cvt_pk_bf16_f32 v117, v18, v19\n\tv_mfma_f32_32x32x16_bf16 v[32:47], v[144:147], v[120:123], v[32:47]\n\tbuffer_load_dwordx4 v[136:139], %[vo], %[rs], %[s0] offen offset:1024 sc1\n\t" \
    "v_exp_f32 v24, v24\n\tv_exp_f32 v25, v25\n\tv_cvt_pk_bf16_f32 v118, v20, v21\n\tv_exp_f32 v26, v26\n\t" \
    "v_exp_f32 v27, v27\n\tv_cvt_pk_bf16_f32 v119, v22, v23\n\tv_exp_f32 v128, v28\n\tv_exp_f32 v129, v29\n\t" \
    "v_cvt_pk_bf16_f32 v124, v24, v25\n\tv_mfma_f32_32x32x16_bf16 v[48:63], v[140:143], v[116:119], v[48:63]\n\tv_exp_f32 v130, v30\n\tv_exp_f32 v131, v31\n\t" \
    "v_cvt_pk_bf16_f32 v125, v26, v27\n\tv_mfma_f32_32x32x16_bf16 v[16:31], v[80:83], v[72:75], 0\n\ts_mov_b32 %[s1], 73728\n\tv_exp_f32 v0, v0\n\t" \
    "v_exp_f32 v1, v1\n\tv_cvt_pk_bf16_f32 v126, v128, v129\n\tv_exp_f32 v2, v2\n\tv_exp_f32 v3, v3\n\t" \
    "v_cvt_pk_bf16_f32 v127, v130, v131\n\tv_mfma_f32_32x32x16_bf16 v[16:31], v[84:87], v[76:79], v[16:31]\n\tv_exp_f32 v4, v4\n\tv_exp_f32 v5, v5\n\t" \
    "v_cvt_pk_bf16_f32 v112, v0, v1\n\tv_exp_f32 v6, v6\n\tv_exp_f32 v7, v7\n\tv_cvt_pk_bf16_f32 v113, v2, v3\n\t" \
    "v_mfma_f32_32x32x16_bf16 v[48:63], v[144:147], v[124:127], v[48:63]\n\ts_waitcnt vmcnt(4)\n\tv_exp_f32 v8, v8\n\tv_exp_f32 v9, v9\n\t" \
    "v_cvt_pk_bf16_f32 v114, v4, v5\n\tv_exp_f32 v10, v10\n\tv_exp_f32 v11, v11\n\tv_cvt_pk_bf16_f32 v115, v6, v7\n\t" \
    "v_exp_f32 v128, v12\n\tv_exp_f32 v129, v13\n\tv_cvt_pk_bf16_f32 v120, v8, v9\n\tv_mfma_f32_32x32x16_bf16 v[32:47], v[96:99], v[112:115], v[32:47]\n\t" \
    "buffer_load_dwordx4 v[140:143], %[vo], %[rs], %[s0] offen offset:2048 sc1\n\tv_exp_f32 v130, v14\n\tv_exp_f32 v131, v15\n\tv_cvt_pk_bf16_f32 v121, v10, v11\n\t" \
    "v_mfma_f32_32x32x16_bf16 v[0:15], v[88:91], v[64:67], 0\n\tbuffer_load_dwordx4 v[144:147], %[vo], %[rs], %[s0] offen offset:3072 sc1\n\tv_exp_f32 v16, v16\n\tv_exp_f32 v17, v17\n\t" \
    "v_cvt_pk_bf16_f32 v122, v128, v129\n\tv_exp_f32 v18, v18\n\tv_exp_f32 v19, v19\n\tv_cvt_pk_bf16_f32 v123, v130, v131\n\t" \
    "v_mfma_f32_32x32x16_bf16 v[0:15], v[92:95], v[68:71], v[0:15]\n\tbuffer_load_dwordx4 v[80:83], %[vo], %[rs], %[s1] offen sc1\n\tv_exp_f32 v20, v20\n\tv_exp_f32 v21, v21\n\t" \
    "v_cvt_pk_bf16_f32 v116, v16, v17\n\tv_exp_f32 v22, v22\n\tv_exp_f32 v23, v23\n\tv_cvt_pk_bf16_f32 v117, v18, v19\n\t" \
    "v_mfma_f32_32x32x16_bf16 v[32:47], v[100:103], v[120:123], v[32:47]\n\tbuffer_load_dwordx4 v[84:87], %[vo], %[rs], %[s1] offen offset:1024 sc1\n\tv_exp_f32 v24, v24\n\tv_exp_f32 v25, v25\n\t" \
    "v_cvt_pk_bf16_f32 v118, v20, v21\n\tv_exp_f32 v26, v26\n\tv_exp_f32 v27, v27\n\tv_cvt_pk_bf16_f32 v119, v22, v23\n\t" \
    "v_exp_f32 v128, v28\n\tv_exp_f32 v129, v29\n\tv_cvt_pk_bf16_f32 v124, v24, v25\n\tv_mfma_f32_32x32x16_bf16 v[48:63], v[96:99], v[116:119], v[48:63]\n\t" \
    "v_exp_f32 v130, v30\n\tv_exp_f32 v131, v31\n\tv_cvt_pk_bf16_f32 v125, v26, v27\n\tv_mfma_f32_32x32x16_bf16 v[16:31], v[88:91], v[72:75], 0\n\t" \
    "s_mov_b32 %[s0], 139264\n\tv_exp_f32 v0, v0\n\tv_exp_f32 v1, v1\n\tv_cvt_pk_bf16_f32 v126, v128, v129\n\t" \
    "v_exp_f32 v2, v2\n\tv_exp_f32 v3, v3\n\tv_cvt_pk_bf16_f32 v127, v130, v131\n\tv_mfma_f32_32x32x16_bf16 v[16:31], v[92:95], v[76:79], v[16:31]\n\t" \
    "v_exp_f32 v4, v4\n\tv_exp_f32 v5, v5\n\tv_cvt_pk_bf16_f32 v112, v0, v1\n\tv_exp_f32 v6, v6\n\t" \
    "v_exp_f32 v7, v7\n\tv_cvt_pk_bf16_f32 v113, v2, v3\n\tv_mfma_f32_32x32x16_bf16 v[48:63], v[100:103], v[124:127], v[48:63]\n\ts_waitcnt vmcnt(4)\n\t" \
    "v_exp_f32 v8, v8\n\tv_exp_f32 v9, v9\n\tv_cvt_pk_bf16_f32 v114, v4, v5\n\tv_exp_f32 v10, v10\n\t" \
    "v_exp_f32 v11, v11\n\tv_cvt_pk_bf16_f32 v115, v6, v7\n\tv_exp_f32 v128, v12\n\tv_exp_f32 v129, v13\n\t" \
    "v_cvt_pk_bf16_f32 v120, v8, v9\n\tv_mfma_f32_32x32x16_bf16 v[32:47], v[104:107], v[112:115], v[32:47]\n\tbuffer_load_dwordx4 v[96:99], %[vo], %[rs], %[s1] offen offset:2048 sc1\n\tv_exp_f32 v130, v14\n\t" \
    "v_exp_f32 v131, v15\n\tv_cvt_pk_bf16_f32 v121, v10, v11\n\tv_mfma_f32_32x32x16_bf16 v[0:15], v[132:135], v[64:67], 0\n\tbuffer_load_dwordx4 v[100:103], %[vo], %[rs], %[s1] offen offset:3072 sc1\n\t" \
    "v_exp_f32 v16, v16\n\tv_exp_f32 v17, v17\n\tv_cvt_pk_bf16_f32 v122, v128, v129\n\tv_exp_f32 v18, v18\n\t" \
    "v_exp_f32 v19, v19\n\tv_cvt_pk_bf16_f32 v123, v130, v131\n\tv_mfma_f32_32x32x16_bf16 v[0:15], v[136:139], v[68:71], v[0:15]\n\tbuffer_load_dwordx4 v[88:91], %[vo], %[rs], %[s0] offen sc1\n\t" \
    "v_exp_f32 v20, v20\n\tv_exp_f32 v21, v21\n\tv_cvt_pk_bf16_f32 v116, v16, v17\n\tv_exp_f32 v22, v22\n\t" \
    "v_exp_f32 v23, v23\n\tv_cvt_pk_bf16_f32 v117, v18, v19\n\tv_mfma_f32_32x32x16_bf16 v[32:47], v[108:111], v[120:123], v[32:47]\n\tbuffer_load_dwordx4 v[92:95], %[vo], %[rs], %[s0] offen offset:1024 sc1\n\t" \
    "v_exp_f32 v24, v24\n\tv_exp_f32 v25, v25\n\tv_cvt_pk_bf16_f32 v118, v20, v21\n\tv_exp_f32 v26, v26\n\t" \
    "v_exp_f32 v27, v27\n\tv_cvt_pk_bf16_f32 v119, v22, v23\n\tv_exp_f32 v128, v28\n\tv_exp_f32 v129, v29\n\t" \
    "v_cvt_pk_bf16_f32 v124, v24, v25\n\tv_mfma_f32_32x32x16_bf16 v[48:63], v[104:107], v[116:119], v[48:63]\n\tv_exp_f32 v130, v30\n\tv_exp_f32 v131, v31\n\t" \
    "v_cvt_pk_bf16_f32 v125, v26, v27\n\tv_mfma_f32_32x32x16_bf16 v[16:31], v[132:135], v[72:75], 0\n\ts_mov_b32 %[s1], 204800\n\tv_exp_f32 v0, v0\n\t" \
    "v_exp_f32 v1, v1\n\tv_cvt_pk_bf16_f32 v126, v128, v129\n\tv_exp_f32 v2, v2\n\tv_exp_f32 v3, v3\n\t" \
    "v_cvt_pk_bf16_f32 v127, v130, v131\n\tv_mfma_f32_32x32x16_bf16 v[16:31], v[136:139], v[76:79], v[16:31]\n\tv_exp_f32 v4, v4\n\tv_exp_f32 v5, v5\n\t" \
    "v_cvt_pk_bf16_f32 v112, v0, v1\n\tv_exp_f32 v6, v6\n\tv_exp_f32 v7, v7\n\tv_cvt_pk_bf16_f32 v113, v2, v3\n\t" \
    "v_mfma_f32_32x32x16_bf16 v[48:63], v[108:111], v[124:127], v[48:63]\n\ts_waitcnt vmcnt(4)\n\tv_exp_f32 v8, v8\n\tv_exp_f32 v9, v9\n\t" \
    "v_cvt_pk_bf16_f32 v114, v4, v5\n\tv_exp_f32 v10, v10\n\tv_exp_f32 v11, v11\n\tv_cvt_pk_bf16_f32 v115, v6, v7\n\t" \
    "v_exp_f32 v128, v12\n\tv_exp_f32 v129, v13\n\tv_cvt_pk_bf16_f32 v120, v8, v9\n\tv_mfma_f32_32x32x16_bf16 v[32:47], v[140:143], v[112:115], v[32:47]\n\t" \
    "buffer_load_dwordx4 v[104:107], %[vo], %[rs], %[s0] offen offset:2048 sc1\n\tv_exp_f32 v130, v14\n\tv_exp_f32 v131, v15\n\tv_cvt_pk_bf16_f32 v121, v10, v11\n\t" \
    "v_mfma_f32_32x32x16_bf16 v[0:15], v[80:83], v[64:67], 0\n\tbuffer_load_dwordx4 v[108:111], %[vo], %[rs], %[s0] offen offset:3072 sc1\n\tv_exp_f32 v16, v16\n\tv_exp_f32 v17, v17\n\t" \
    "v_cvt_pk_bf16_f32 v122, v128, v129\n\tv_exp_f32 v18, v18\n\tv_exp_f32 v19, v19\n\tv_cvt_pk_bf16_f32 v123, v130, v131\n\t" \
    "v_mfma_f32_32x32x16_bf16 v[0:15], v[84:87], v[68:71], v[0:15]\n\tbuffer_load_dwordx4 v[132:135], %[vo], %[rs], %[s1] offen sc1\n\tv_exp_f32 v20, v20\n\tv_exp_f32 v21, v21\n\t" \
    "v_cvt_pk_bf16_f32 v116, v16, v17\n\tv_exp_f32 v22, v22\n\tv_exp_f32 v23, v23\n\tv_cvt_pk_bf16_f32 v117, v18, v19\n\t" \
    "v_mfma_f32_32x32x16_bf16 v[32:47], v[144:147], v[120:123], v[32:47]\n\tbuffer_load_dwordx4 v[136:139], %[vo], %[rs], %[s1] offen offset:1024 sc1\n\tv_exp_f32 v24, v24\n\tv_exp_f32 v25, v25\n\t" \
    "v_cvt_pk_bf16_f32 v118, v20, v21\n\tv_exp_f32 v26, v26\n\tv_exp_f32 v27, v27\n\tv_cvt_pk_bf16_f32 v119, v22, v23\n\t" \
    "v_exp_f32 v128, v28\n\tv_exp_f32 v129, v29\n\tv_cvt_pk_bf16_f32 v124, v24, v25\n\tv_mfma_f32_32x32x16_bf16 v[48:63], v[140:143], v[116:119], v[48:63]\n\t" \
    "v_exp_f32 v130, v30\n\tv_exp_f32 v131, v31\n\tv_cvt_pk_bf16_f32 v125, v26, v27\n\tv_mfma_f32_32x32x16_bf16 v[16:31], v[80:83], v[72:75], 0\n\t" \
    "s_mov_b32 %[s0], 12288\n\tv_exp_f32 v0, v0\n\tv_exp_f32 v1, v1\n\tv_cvt_pk_bf16_f32 v126, v128, v129\n\t" \
    "v_exp_f32 v2, v2\n\tv_exp_f32 v3, v3\n\tv_cvt_pk_bf16_f32 v127, v130, v131\n\tv_mfma_f32_32x32x16_bf16 v[16:31], v[84:87], v[76:79], v[16:31]\n\t" \
    "v_exp_f32 v4, v4\n\tv_exp_f32 v5, v5\n\tv_cvt_pk_bf16_f32 v112, v0, v1\n\tv_exp_f32 v6, v6\n\t" \
    "v_exp_f32 v7, v7\n\tv_cvt_pk_bf16_f32 v113, v2, v3\n\tv_mfma_f32_32x32x16_bf16 v[48:63], v[144:147], v[124:127], v[48:63]\n\ts_waitcnt vmcnt(4)\n\t" \
    "v_exp_f32 v8, v8\n\tv_exp_f32 v9, v9\n\tv_cvt_pk_bf16_f32 v114, v4, v5\n\tv_exp_f32 v10, v10\n\t" \
    "v_exp_f32 v11, v11\n\tv_cvt_pk_bf16_f32 v115, v6, v7\n\tv_exp_f32 v128, v12\n\tv_exp_f32 v129, v13\n\t" \
    "v_cvt_pk_bf16_f32 v120, v8, v9\n\tv_mfma_f32_32x32x16_bf16 v[32:47], v[96:99], v[112:115], v[32:47]\n\tbuffer_load_dwordx4 v[140:143], %[vo], %[rs], %[s1] offen offset:2048 sc1\n\tv_exp_f32 v130, v14\n\t" \
    "v_exp_f32 v131, v15\n\tv_cvt_pk_bf16_f32 v121, v10, v11\n\tv_mfma_f32_32x32x16_bf16 v[0:15], v[88:91], v[64:67], 0\n\tbuffer_load_dwordx4 v[144:147], %[vo], %[rs], %[s1] offen offset:3072 sc1\n\t" \
    "v_exp_f32 v16, v16\n\tv_exp_f32 v17, v17\n\tv_cvt_pk_bf16_f32 v122, v128, v129\n\tv_exp_f32 v18, v18\n\t" \
    "v_exp_f32 v19, v19\n\tv_cvt_pk_bf16_f32 v123, v130, v131\n\tv_mfma_f32_32x32x16_bf16 v[0:15], v[92:95], v[68:71], v[0:15]\n\tbuffer_load_dwordx4 v[80:83], %[vo], %[rs], %[s0] offen sc1\n\t" \
    "v_exp_f32 v20, v20\n\tv_exp_f32 v21, v21\n\tv_cvt_pk_bf16_f32 v116, v16, v17\n\tv_exp_f32 v22, v22\n\t" \
    "v_exp_f32 v23, v23\n\tv_cvt_pk_bf16_f32 v117, v18, v19\n\tv_mfma_f32_32x32x16_bf16 v[32:47], v[100:103], v[120:123], v[32:47]\n\tbuffer_load_dwordx4 v[84:87], %[vo], %[rs], %[s0] offen offset:1024 sc1\n\t" \
    "v_exp_f32 v24, v24\n\tv_exp_f32 v25, v25\n\tv_cvt_pk_bf16_f32 v118, v20, v21\n\tv_exp_f32 v26, v26\n\t" \
    "v_exp_f32 v27, v27\n\tv_cvt_pk_bf16_f32 v119, v22, v23\n\tv_exp_f32 v128, v28\n\tv_exp_f32 v129, v29\n\t" \
    "v_cvt_pk_bf16_f32 v124, v24, v25\n\tv_mfma_f32_32x32x16_bf16 v[48:63], v[96:99], v[116:119], v[48:63]\n\tv_exp_f32 v130, v30\n\tv_exp_f32 v131, v31\n\t" \
    "v_cvt_pk_bf16_f32 v125, v26, v27\n\tv_mfma_f32_32x32x16_bf16 v[16:31], v[88:91], v[72:75], 0\n\ts_mov_b32 %[s1], 77824\n\tv_exp_f32 v0, v0\n\t" \
    "v_exp_f32 v1, v1\n\tv_cvt_pk_bf16_f32 v126, v128, v129\n\tv_exp_f32 v2, v2\n\tv_exp_f32 v3, v3\n\t" \
    "v_cvt_pk_bf16_f32 v127, v130, v131\n\tv_mfma_f32_32x32x16_bf16 v[16:31], v[92:95], v[76:79], v[16:31]\n\tv_exp_f32 v4, v4\n\tv_exp_f32 v5, v5\n\t" \
    "v_cvt_pk_bf16_f32 v112, v0, v1\n\tv_exp_f32 v6, v6\n\tv_exp_f32 v7, v7\n\tv_cvt_pk_bf16_f32 v113, v2, v3\n\t" \
    "v_mfma_f32_32x32x16_bf16 v[48:63], v[100:103], v[124:127], v[48:63]\n\ts_waitcnt vmcnt(4)\n\tv_exp_f32 v8, v8\n\tv_exp_f32 v9, v9\n\t" \
    "v_cvt_pk_bf16_f32 v114, v4, v5\n\tv_exp_f32 v10, v10\n\tv_exp_f32 v11, v11\n\tv_cvt_pk_bf16_f32 v115, v6, v7\n\t" \
    "v_exp_f32 v128, v12\n\tv_exp_f32 v129, v13\n\tv_cvt_pk_bf16_f32 v120, v8, v9\n\tv_mfma_f32_32x32x16_bf16 v[32:47], v[104:107], v[112:115], v[32:47]\n\t" \
    "buffer_load_dwordx4 v[96:99], %[vo], %[rs], %[s0] offen offset:2048 sc1\n\tv_exp_f32 v130, v14\n\tv_exp_f32 v131, v15\n\tv_cvt_pk_bf16_f32 v121, v10, v11\n\t" \
    "v_mfma_f32_32x32x16_bf16 v[0:15], v[132:135], v[64:67], 0\n\tbuffer_load_dwordx4 v[100:103], %[vo], %[rs], %[s0] offen offset:3072 sc1\n\tv_exp_f32 v16, v16\n\tv_exp_f32 v17, v17\n\t" \
    "v_cvt_pk_bf16_f32 v122, v128, v129\n\tv_exp_f32 v18, v18\n\tv_exp_f32 v19, v19\n\tv_cvt_pk_bf16_f32 v123, v130, v131\n\t" \
    "v_mfma_f32_32x32x16_bf16 v[0:15], v[136:139], v[68:71], v[0:15]\n\tbuffer_load_dwordx4 v[88:91], %[vo], %[rs], %[s1] offen sc1\n\tv_exp_f32 v20, v20\n\tv_exp_f32 v21, v21\n\t" \
    "v_cvt_pk_bf16_f32 v116, v16, v17\n\tv_exp_f32 v22, v22\n\tv_exp_f32 v23, v23\n\tv_cvt_pk_bf16_f32 v117, v18, v19\n\t" \
    "v_mfma_f32_32x32x16_bf16 v[32:47], v[108:111], v[120:123], v[32:47]\n\tbuffer_load_dwordx4 v[92:95], %[vo], %[rs], %[s1] offen offset:1024 sc1\n\tv_exp_f32 v24, v24\n\tv_exp_f32 v25, v25\n\t" \
    "v_cvt_pk_bf16_f32 v118, v20, v21\n\tv_exp_f32 v26, v26\n\tv_exp_f32 v27, v27\n\tv_cvt_pk_bf16_f32 v119, v22, v23\n\t" \
    "v_exp_f32 v128, v28\n\tv_exp_f32 v129, v29\n\tv_cvt_pk_bf16_f32 v124, v24, v25\n\tv_mfma_f32_32x32x16_bf16 v[48:63], v[104:107], v[116:119], v[48:63]\n\t" \
    "v_exp_f32 v130, v30\n\tv_exp_f32 v131, v31\n\tv_cvt_pk_bf16_f32 v125, v26, v27\n\tv_mfma_f32_32x32x16_bf16 v[16:31], v[132:135], v[72:75], 0\n\t" \
    "s_mov_b32 %[s0], 143360\n\tv_exp_f32 v0, v0\n\tv_exp_f32 v1, v1\n\tv_cvt_pk_bf16_f32 v126, v128, v129\n\t" \
    "v_exp_f32 v2, v2\n\tv_exp_f32 v3, v3\n\tv_cvt_pk_bf16_f32 v127, v130, v131\n\tv_mfma_f32_32x32x16_bf16 v[16:31], v[136:139], v[76:79], v[16:31]\n\t" \
    "v_exp_f32 v4, v4\n\tv_exp_f32 v5, v5\n\tv_cvt_pk_bf16_f32 v112, v0, v1\n\tv_exp_f32 v6, v6\n\t" \
    "v_exp_f32 v7, v7\n\tv_cvt_pk_bf16_f32 v113, v2, v3\n\tv_mfma_f32_32x32x16_bf16 v[48:63], v[108:111], v[124:127], v[48:63]\n\ts_waitcnt vmcnt(4)\n\t" \
    "v_exp_f32 v8, v8\n\tv_exp_f32 v9, v9\n\tv_cvt_pk_bf16_f32 v114, v4, v5\n\tv_exp_f32 v10, v10\n\t" \
    "v_exp_f32 v11, v11\n\tv_cvt_pk_bf16_f32 v115, v6, v7\n\tv_exp_f32 v128, v12\n\tv_exp_f32 v129, v13\n\t" \
    "v_cvt_pk_bf16_f32 v120, v8, v9\n\tv_mfma_f32_32x32x16_bf16 v[32:47], v[140:143], v[112:115], v[32:47]\n\tbuffer_load_dwordx4 v[104:107], %[vo], %[rs], %[s1] offen offset:2048 sc1\n\tv_exp_f32 v130, v14\n\t" \
    "v_exp_f32 v131, v15\n\tv_cvt_pk_bf16_f32 v121, v10, v11\n\tv_mfma_f32_32x32x16_bf16 v[0:15], v[80:83], v[64:67], 0\n\tbuffer_load_dwordx4 v[108:111], %[vo], %[rs], %[s1] offen offset:3072 sc1\n\t" \
    "v_exp_f32 v16, v16\n\tv_exp_f32 v17, v17\n\tv_cvt_pk_bf16_f32 v122, v128, v129\n\tv_exp_f32 v18, v18\n\t" \
    "v_exp_f32 v19, v19\n\tv_cvt_pk_bf16_f32 v123, v130, v131\n\tv_mfma_f32_32x32x16_bf16 v[0:15], v[84:87], v[68:71], v[0:15]\n\tbuffer_load_dwordx4 v[132:135], %[vo], %[rs], %[s0] offen sc1\n\t" \
    "v_exp_f32 v20, v20\n\tv_exp_f32 v21, v21\n\tv_cvt_pk_bf16_f32 v116, v16, v17\n\tv_exp_f32 v22, v22\n\t" \
    "v_exp_f32 v23, v23\n\tv_cvt_pk_bf16_f32 v117, v18, v19\n\tv_mfma_f32_32x32x16_bf16 v[32:47], v[144:147], v[120:123], v[32:47]\n\tbuffer_load_dwordx4 v[136:139], %[vo], %[rs], %[s0] offen offset:1024 sc1\n\t" \
    "v_exp_f32 v24, v24\n\tv_exp_f32 v25, v25\n\tv_cvt_pk_bf16_f32 v118, v20, v21\n\tv_exp_f32 v26, v26\n\t" \
    "v_exp_f32 v27, v27\n\tv_cvt_pk_bf16_f32 v119, v22, v23\n\tv_exp_f32 v128, v28\n\tv_exp_f32 v129, v29\n\t" \
    "v_cvt_pk_bf16_f32 v124, v24, v25\n\tv_mfma_f32_32x32x16_bf16 v[48:63], v[140:143], v[116:119], v[48:63]\n\tv_exp_f32 v130, v30\n\tv_exp_f32 v131, v31\n\t" \
    "v_cvt_pk_bf16_f32 v125, v26, v27\n\tv_mfma_f32_32x32x16_bf16 v[16:31], v[80:83], v[72:75], 0\n\ts_mov_b32 %[s1], 208896\n\tv_exp_f32 v0, v0\n\t" \
    "v_exp_f32 v1, v1\n\tv_cvt_pk_bf16_f32 v126, v128, v129\n\tv_exp_f32 v2, v2\n\tv_exp_f32 v3, v3\n\t" \
    "v_cvt_pk_bf16_f32 v127, v130, v131\n\tv_mfma_f32_32x32x16_bf16 v[16:31], v[84:87], v[76:79], v[16:31]\n\tv_exp_f32 v4, v4\n\tv_exp_f32 v5, v5\n\t" \
    "v_cvt_pk_bf16_f32 v112, v0, v1\n\tv_exp_f32 v6, v6\n\tv_exp_f32 v7, v7\n\tv_cvt_pk_bf16_f32 v113, v2, v3\n\t" \
    "v_mfma_f32_32x32x16_bf16 v[48:63], v[144:147], v[124:127], v[48:63]\n\ts_waitcnt vmcnt(4)\n\tv_exp_f32 v8, v8\n\tv_exp_f32 v9, v9\n\t" \
    "v_cvt_pk_bf16_f32 v114, v4, v5\n\tv_exp_f32 v10, v10\n\tv_exp_f32 v11, v11\n\tv_cvt_pk_bf16_f32 v115, v6, v7\n\t" \
    "v_exp_f32 v128, v12\n\tv_exp_f32 v129, v13\n\tv_cvt_pk_bf16_f32 v120, v8, v9\n\tv_mfma_f32_32x32x16_bf16 v[32:47], v[96:99], v[112:115], v[32:47]\n\t" \
    "buffer_load_dwordx4 v[140:143], %[vo], %[rs], %[s0] offen offset:2048 sc1\n\tv_exp_f32 v130, v14\n\tv_exp_f32 v131, v15\n\tv_cvt_pk_bf16_f32 v121, v10, v11\n\t" \
    "v_mfma_f32_32x32x16_bf16 v[0:15], v[88:91], v[64:67], 0\n\tbuffer_load_dwordx4 v[144:147], %[vo], %[rs], %[s0] offen offset:3072 sc1\n\tv_exp_f32 v16, v16\n\tv_exp_f32 v17, v17\n\t" \
    "v_cvt_pk_bf16_f32 v122, v128, v129\n\tv_exp_f32 v18, v18\n\tv_exp_f32 v19, v19\n\tv_cvt_pk_bf16_f32 v123, v130, v131\n\t" \
    "v_mfma_f32_32x32x16_bf16 v[0:15], v[92:95], v[68:71], v[0:15]\n\tbuffer_load_dwordx4 v[80:83], %[vo], %[rs], %[s1] offen sc1\n\tv_exp_f32 v20, v20\n\tv_exp_f32 v21, v21\n\t" \
    "v_cvt_pk_bf16_f32 v116, v16, v17\n\tv_exp_f32 v22, v22\n\tv_exp_f32 v23, v23\n\tv_cvt_pk_bf16_f32 v117, v18, v19\n\t" \
    "v_mfma_f32_32x32x16_bf16 v[32:47], v[100:103], v[120:123], v[32:47]\n\tbuffer_load_dwordx4 v[84:87], %[vo], %[rs], %[s1] offen offset:1024 sc1\n\tv_exp_f32 v24, v24\n\tv_exp_f32 v25, v25\n\t" \
    "v_cvt_pk_bf16_f32 v118, v20, v21\n\tv_exp_f32 v26, v26\n\tv_exp_f32 v27, v27\n\tv_cvt_pk_bf16_f32 v119, v22, v23\n\t" \
    "v_exp_f32 v128, v28\n\tv_exp_f32 v129, v29\n\tv_cvt_pk_bf16_f32 v124, v24, v25\n\tv_mfma_f32_32x32x16_bf16 v[48:63], v[96:99], v[116:119], v[48:63]\n\t" \
    "v_exp_f32 v130, v30\n\tv_exp_f32 v131, v31\n\tv_cvt_pk_bf16_f32 v125, v26, v27\n\tv_mfma_f32_32x32x16_bf16 v[16:31], v[88:91], v[72:75], 0\n\t" \
    "v_exp_f32 v0, v0\n\tv_exp_f32 v1, v1\n\tv_cvt_pk_bf16_f32 v126, v128, v129\n\tv_exp_f32 v2, v2\n\t" \
    "v_exp_f32 v3, v3\n\tv_cvt_pk_bf16_f32 v127, v130, v131\n\tv_mfma_f32_32x32x16_bf16 v[16:31], v[92:95], v[76:79], v[16:31]\n\tv_exp_f32 v4, v4\n\t" \
    "v_exp_f32 v5, v5\n\tv_cvt_pk_bf16_f32 v112, v0, v1\n\tv_exp_f32 v6, v6\n\tv_exp_f32 v7, v7\n\t" \
    "v_cvt_pk_bf16_f32 v113, v2, v3\n\tv_mfma_f32_32x32x16_bf16 v[48:63], v[100:103], v[124:127], v[48:63]\n\ts_waitcnt vmcnt(4)\n\tv_exp_f32 v8, v8\n\t" \
    "v_exp_f32 v9, v9\n\tv_cvt_pk_bf16_f32 v114, v4, v5\n\tv_exp_f32 v10, v10\n\tv_exp_f32 v11, v11\n\t" \
    "v_cvt_pk_bf16_f32 v115, v6, v7\n\tv_exp_f32 v128, v12\n\tv_exp_f32 v129, v13\n\tv_cvt_pk_bf16_f32 v120, v8, v9\n\t" \
    "v_mfma_f32_32x32x16_bf16 v[32:47], v[104:107], v[112:115], v[32:47]\n\tbuffer_load_dwordx4 v[96:99], %[vo], %[rs], %[s1] offen offset:2048 sc1\n\tv_exp_f32 v130, v14\n\tv_exp_f32 v131, v15\n\t" \
    "v_cvt_pk_bf16_f32 v121, v10, v11\n\tv_mfma_f32_32x32x16_bf16 v[0:15], v[132:135], v[64:67], 0\n\tbuffer_load_dwordx4 v[100:103], %[vo], %[rs], %[s1] offen offset:3072 sc1\n\tv_exp_f32 v16, v16\n\t" \
    "v_exp_f32 v17, v17\n\tv_cvt_pk_bf16_f32 v122, v128, v129\n\tv_exp_f32 v18, v18\n\tv_exp_f32 v19, v19\n\t" \
    "v_cvt_pk_bf16_f32 v123, v130, v131\n\tv_mfma_f32_32x32x16_bf16 v[0:15], v[136:139], v[68:71], v[0:15]\n\tv_exp_f32 v20, v20\n\tv_exp_f32 v21, v21\n\t" \
    "v_cvt_pk_bf16_f32 v116, v16, v17\n\tv_exp_f32 v22, v22\n\tv_exp_f32 v23, v23\n\tv_cvt_pk_bf16_f32 v117, v18, v19\n\t" \
    "v_mfma_f32_32x32x16_bf16 v[32:47], v[108:111], v[120:123], v[32:47]\n\tv_exp_f32 v24, v24\n\tv_exp_f32 v25, v25\n\tv_cvt_pk_bf16_f32 v118, v20, v21\n\t" \
    "v_exp_f32 v26, v26\n\tv_exp_f32 v27, v27\n\tv_cvt_pk_bf16_f32 v119, v22, v23\n\tv_exp_f32 v128, v28\n\t" \
    "v_exp_f32 v129, v29\n\tv_cvt_pk_bf16_f32 v124, v24, v25\n\tv_mfma_f32_32x32x16_bf16 v[48:63], v[104:107], v[116:119], v[48:63]\n\tv_exp_f32 v130, v30\n\t" \
    "v_exp_f32 v131, v31\n\tv_cvt_pk_bf16_f32 v125, v26, v27\n\tv_mfma_f32_32x32x16_bf16 v[16:31], v[132:135], v[72:75], 0\n\tv_exp_f32 v0, v0\n\t" \
    "v_exp_f32 v1, v1\n\tv_cvt_pk_bf16_f32 v126, v128, v129\n\tv_exp_f32 v2, v2\n\tv_exp_f32 v3, v3\n\t" \
    "v_cvt_pk_bf16_f32 v127, v130, v131\n\tv_mfma_f32_32x32x16_bf16 v[16:31], v[136:139], v[76:79], v[16:31]\n\tv_exp_f32 v4, v4\n\tv_exp_f32 v5, v5\n\t" \
    "v_cvt_pk_bf16_f32 v112, v0, v1\n\tv_exp_f32 v6, v6\n\tv_exp_f32 v7, v7\n\tv_cvt_pk_bf16_f32 v113, v2, v3\n\t" \
    "v_mfma_f32_32x32x16_bf16 v[48:63], v[108:111], v[124:127], v[48:63]\n\ts_waitcnt vmcnt(2)\n\tv_exp_f32 v8, v8\n\tv_exp_f32 v9, v9\n\t" \
    "v_cvt_pk_bf16_f32 v114, v4, v5\n\tv_exp_f32 v10, v10\n\tv_exp_f32 v11, v11\n\tv_cvt_pk_bf16_f32 v115, v6, v7\n\t" \
    "v_exp_f32 v128, v12\n\tv_exp_f32 v129, v13\n\tv_cvt_pk_bf16_f32 v120, v8, v9\n\tv_mfma_f32_32x32x16_bf16 v[32:47], v[140:143], v[112:115], v[32:47]\n\t" \
    "v_exp_f32 v130, v14\n\tv_exp_f32 v131, v15\n\tv_cvt_pk_bf16_f32 v121, v10, v11\n\tv_mfma_f32_32x32x16_bf16 v[0:15], v[80:83], v[64:67], 0\n\t" \
    "v_exp_f32 v16, v16\n\tv_exp_f32 v17, v17\n\tv_cvt_pk_bf16_f32 v122, v128, v129\n\tv_exp_f32 v18, v18\n\t" \
    "v_exp_f32 v19, v19\n\tv_cvt_pk_bf16_f32 v123, v130, v131\n\tv_mfma_f32_32x32x16_bf16 v[0:15], v[84:87], v[68:71], v[0:15]\n\tv_exp_f32 v20, v20\n\t" \
    "v_exp_f32 v21, v21\n\tv_cvt_pk_bf16_f32 v116, v16, v17\n\tv_exp_f32 v22, v22\n\tv_exp_f32 v23, v23\n\t" \
    "v_cvt_pk_bf16_f32 v117, v18, v19\n\tv_mfma_f32_32x32x16_bf16 v[32:47], v[144:147], v[120:123], v[32:47]\n\tv_exp_f32 v24, v24\n\tv_exp_f32 v25, v25\n\t" \
    "v_cvt_pk_bf16_f32 v118, v20, v21\n\tv_exp_f32 v26, v26\n\tv_exp_f32 v27, v27\n\tv_cvt_pk_bf16_f32 v119, v22, v23\n\t" \
    "v_exp_f32 v128, v28\n\tv_exp_f32 v129, v29\n\tv_cvt_pk_bf16_f32 v124, v24, v25\n\tv_mfma_f32_32x32x16_bf16 v[48:63], v[140:143], v[116:119], v[48:63]\n\t" \
    "v_exp_f32 v130, v30\n\tv_exp_f32 v131, v31\n\tv_cvt_pk_bf16_f32 v125, v26, v27\n\tv_mfma_f32_32x32x16_bf16 v[16:31], v[80:83], v[72:75], 0\n\t" \
    "v_exp_f32 v0, v0\n\tv_exp_f32 v1, v1\n\tv_cvt_pk_bf16_f32 v126, v128, v129\n\tv_exp_f32 v2, v2\n\t" \
    "v_exp_f32 v3, v3\n\tv_cvt_pk_bf16_f32 v127, v130, v131\n\tv_mfma_f32_32x32x16_bf16 v[16:31], v[84:87], v[76:79], v[16:31]\n\tv_exp_f32 v4, v4\n\t" \
    "v_exp_f32 v5, v5\n\tv_cvt_pk_bf16_f32 v112, v0, v1\n\tv_exp_f32 v6, v6\n\tv_exp_f32 v7, v7\n\t" \
    "v_cvt_pk_bf16_f32 v113, v2, v3\n\tv_mfma_f32_32x32x16_bf16 v[48:63], v[144:147], v[124:127], v[48:63]\n\ts_waitcnt vmcnt(0)\n\tv_exp_f32 v8, v8\n\t" \
    "v_exp_f32 v9, v9\n\tv_cvt_pk_bf16_f32 v114, v4, v5\n\tv_exp_f32 v10, v10\n\tv_exp_f32 v11, v11\n\t" \
    "v_cvt_pk_bf16_f32 v115, v6, v7\n\tv_exp_f32 v128, v12\n\tv_exp_f32 v129, v13\n\tv_cvt_pk_bf16_f32 v120, v8, v9\n\t" \
    "v_mfma_f32_32x32x16_bf16 v[32:47], v[96:99], v[112:115], v[32:47]\n\tv_exp_f32 v130, v14\n\tv_exp_f32 v131, v15\n\tv_cvt_pk_bf16_f32 v121, v10, v11\n\t" \
    "v_exp_f32 v16, v16\n\tv_exp_f32 v17, v17\n\tv_cvt_pk_bf16_f32 v122, v128, v129\n\tv_exp_f32 v18, v18\n\t" \
    "v_exp_f32 v19, v19\n\tv_cvt_pk_bf16_f32 v123, v130, v131\n\tv_exp_f32 v20, v20\n\tv_exp_f32 v21, v21\n\t" \
    "v_cvt_pk_bf16_f32 v116, v16, v17\n\tv_exp_f32 v22, v22\n\tv_exp_f32 v23, v23\n\tv_cvt_pk_bf16_f32 v117, v18, v19\n\t" \
    "v_mfma_f32_32x32x16_bf16 v[32:47], v[100:103], v[120:123], v[32:47]\n\tv_exp_f32 v24, v24\n\tv_exp_f32 v25, v25\n\tv_cvt_pk_bf16_f32 v118, v20, v21\n\t" \
    "v_exp_f32 v26, v26\n\tv_exp_f32 v27, v27\n\tv_cvt_pk_bf16_f32 v119, v22, v23\n\tv_exp_f32 v128, v28\n\t" \
    "v_exp_f32 v129, v29\n\tv_cvt_pk_bf16_f32 v124, v24, v25\n\tv_mfma_f32_32x32x16_bf16 v[48:63], v[96:99], v[116:119], v[48:63]\n\tv_exp_f32 v130, v30\n\t" \
    "v_exp_f32 v131, v31\n\tv_cvt_pk_bf16_f32 v125, v26, v27\n\tv_cvt_pk_bf16_f32 v126, v128, v129\n\tv_cvt_pk_bf16_f32 v127, v130, v131\n\t" \
    "s_nop 1\n\tv_mfma_f32_32x32x16_bf16 v[48:63], v[100:103], v[124:127], v[48:63]\n\ts_nop 15\n\t.subsection 1\n\t" \
    ".Ls1_%=:\n\ts_movk_i32 %[n], 0x7fff\n\t.Lp1_%=:\n\tds_read_b32 v148, %[ld]\n\t" \
    "s_sub_u32 %[n], %[n], 1\n\ts_waitcnt lgkmcnt(0)\n\ts_cbranch_scc1 .Lb1_%=\n\tv_cmp_gt_u32 vcc, 2, v148\n\t" \
    "s_cbranch_vccz .Lb1_%=\n\ts_sleep 2\n\ts_branch .Lp1_%=\n\t.subsection 0\n\t"
#define CL10(a) "v" #a "0", "v" #a "1", "v" #a "2", "v" #a "3", "v" #a "4", "v" #a "5", "v" #a "6", "v" #a "7", "v" #a "8", "v" #a "9"

template <bool KT, int S0, int S1, int D, int WS> DEVI void mma_steps(f32x16& acc, bf16x8 (&wf)[28], const lptr (&xp)[4], int xo, lptr wa) {
    bf16x8 xa[28];
#define RD(s) do { if (WS > 0) wf[s] = ldtr2(wa + (s) * WS, wa + (s) * WS + WS / 4); if (WS < 0) wf[s] = ldsr(wa + (s) * 32); xa[s] = ldsr(xp[(s) & 3] + xo + ((s) >> 2) * 4096); } while (0)
#pragma unroll
    for (int s = S0; s < S0 + D && s < S1; ++s) RD(s);
    SBAR();
#pragma unroll
    for (int s = S0; s < S1; ++s) {
        acc = KT ? MFMA32(wf[s], xa[s], acc) : MFMA32(xa[s], wf[s], acc);
        if (s + D < S1) RD(s + D);
        SBAR();
    }
#undef RD
}
typedef unsigned u32x16 __attribute__((ext_vector_type(16)));
#define KPTR(v, i) ((char*)((v)[2 * (i)] | ((uintptr_t)(v)[2 * (i) + 1] << 32)))
__global__ __launch_bounds__(640, 3) void fused(const char*, const char*, const char*, const char*, const char*, const char*, const char*, const char*, char*, char*) {
    extern __shared__ __attribute__((aligned(16))) char lds_[];
    u32x16 ka; u32x4 kb;
    STAMP_DECL; STAMP(0);
    int bid = blockIdx.x, tid = threadIdx.x;
    asm volatile("s_load_dwordx16 %0, %3, 0x0\n\ts_load_dwordx4 %1, %3, 0x40" : "=&s"(ka), "=&s"(kb), "+s"(bid) : "s"(__builtin_amdgcn_kernarg_segment_ptr()));
    asm volatile("" : "+v"(tid));
    __builtin_assume(bid >= 0 && bid < 256 && tid >= 0 && tid < 640);
    const lptr lds = (lptr)lds_, misc = lds + L_MISC; const unsigned lds0 = (unsigned)(uintptr_t)lds_;
    int lane = tid & 63; const int wid = RFL(tid >> 6);
    {
        u64 pc;
        asm volatile("s_getpc_b64 %0\n\ts_mov_b32 m0, %2\n\tv_cmp_le_u32 vcc, 0x240, %3\n\ts_mov_b64 exec, vcc\n\tglobal_load_lds_dword %1, %0\n\ts_mov_b64 exec, -1"
                     : "=&s"(pc) : "v"(min(((bid & 56) << 10) + lane * 128, 33792)), "s"(lds0 + L_MISC + M_JUNK), "v"(tid) : "memory", "vcc");
    }
    asm volatile(".subsection 2\n\t.fill 256, 4, 0xBF800000\n\t.subsection 0");
    *(LAS u32x4*)(misc + lane * 16) = (u32x4){};
    const int h = (bid & 3) * 4 + ((bid >> 3) & 3), qb = ((bid >> 2) & 1) * 8 + (bid >> 5);
    const int c = (int)(0xDCBA98F76E543210ull >> (4 * h)) & 15;
    const unsigned xb = qb * 128 * XROW, hw = c < 14; const bool hasw = c < 14;
    unsigned xe = (lane >> 3) * XROW + (((lane & 7) ^ (lane >> 4)) * 16), xd = xe ^ 64;
    unsigned ss = 8 * XROW + hw * (8 * WROW - 8 * XROW), we = hasw ? (lane >> 3) * WROW + (((lane & 7) ^ ((lane >> 2) & 4)) * 16) : xe, wd_ = hasw ? we : xd;
    unsigned kcs = 128 + hw * (64 * WROW - 128), sw0 = xb + 64 * XROW; sw0 += hw * (896 + c * 128 - sw0);
    unsigned kcl = 4096 << hw, hop = 28672 - hw * 24576;
    unsigned mw = lds0 + L_W + wid * kcl, mw2 = mw + hop, sw = sw0 + wid * kcs, sw2 = sw + 4 * ss, mx = lds0 + xq(0) + wid * 4096, sx = xb + wid * 128;
    unsigned tn = 2048 * XROW - hw * (2048 * XROW - 448 * WROW), one = 1; lptr vm = misc;
    asm volatile("" : "+v"(xe), "+v"(xd), "+v"(we), "+v"(wd_), "+v"(vm), "+v"(one));
    asm volatile("s_waitcnt lgkmcnt(0)\n\ts_barrier" : "+s"(ka), "+s"(kb), "+s"(mw), "+s"(mw2), "+s"(ss), "+s"(sw), "+s"(sw2), "+s"(mx), "+s"(sx), "+s"(tn) :: "memory");
    const char *x = KPTR(ka, 0), *wo = KPTR(ka, 2), *wr = KPTR(ka, 3), *wg = KPTR(ka, 4), *wu = KPTR(ka, 5), *wd = KPTR(ka, 6), *gw = KPTR(ka, 7); char *ws = KPTR(kb, 0), *out = KPTR(kb, 1);
    const rsrc_t rsX = mk(x, 2048 * XROW), rsWq = mk(KPTR(ka, 1), 448 * WROW), rsW = mk(ws, WS_BYTES);
    const int qtile = qb * 4 + (h >> 2), cg = h & 3;
    STAMP(15);
    const rsrc_t rsT = mk((const void*)((hasw ? ka[2] : ka[0]) | ((uintptr_t)(hasw ? ka[3] : ka[1]) << 32)), tn);
    if (__builtin_expect(wid < 4, 1)) {
        dma2<2>(mw, 1024, we, wd_, rsT, sw, ss); dma2<2>(mw2, 1024, we, wd_, rsT, sw2, ss); dma2<2>(mx, 1024, xe, xd, rsX, sx, 8 * XROW);
    } else if (wid < 8) {
        const unsigned j = wid & 1, k = 4 + ((wid >> 1) & 1), p6 = 2 * (wid & 3);
        dma2<2>(lds0 + L_W + k * kcl + j * hop, 1024, we, wd_, rsT, sw0 + k * kcs + 4 * j * ss, ss);
        dma2<1>(lds0 + xq(0) + k * 4096 + j * 2048, 1024, xe, xd, rsX, xb + k * 128 + j * 16 * XROW, 8 * XROW);
        dma2<1>(lds0 + L_W + 6 * kcl + (p6 >> 2) * hop + (p6 & 3) * 1024, 1024, we, wd_, rsT, sw0 + 6 * kcs + p6 * ss, ss);
        dma1(lds0 + xq(0) + 6 * 4096 + (wid & 3) * 1024, j ? xd : xe, rsX, xb + 6 * 128 + (wid & 3) * 8 * XROW);
    } else {
        if (wid == 8) {
            u64 ep;
            asm volatile("global_load_dwordx2 %0, %1, %2 sc1\n\ts_waitcnt vmcnt(0)" : "=&v"(ep) : "v"(0), "s"(ws + WS_EP) : "memory");
            ep += 0x9E3779B97F4A7C15ull;
            *(volatile LAS u64*)(vm + M_TAG) = ep; lset<M_TAGS>(vm, one);
            const u64 tag = (u64)(unsigned)RFL((unsigned)ep) | ((u64)(unsigned)RFL((unsigned)(ep >> 32)) << 32);
            if (c == 14) { if (lane < 56) { dma1(lds0 + L_QI, lane * 16, mk(wr, XROW), 0); dma1(lds0 + L_QI + 1040, lane * 16, mk(gw, XROW), 0); } WAITV(0); lset<M_LV>(vm, one); }
            STAMP(1);
            __builtin_amdgcn_s_setprio(3);
            const int c0 = (7 * h) >> 1, j = lane & 3;
            const int cj = j == 0 ? c0 >> 3 : j == 1 ? (c0 + 3) >> 3 : j == 2 ? 7 + ((28 * h) >> 6) : 7 + ((28 * h + 27) >> 6);
            const unsigned fo = (cj * 16 + (lane >> 2)) * FS, vo = lane * 16;
            lwait<0, 1>(vm + (hasw ? M_Q0S : M_WQ), hasw ? 1 : 2);
            __builtin_amdgcn_s_sleep(PD);
            gwait(ws, WS_FQ0 + fo, tag); lset<M_LAND>(vm, one);
            STAMP(3);
            lwait<M_RS, 1>(vm, 1);
            __builtin_amdgcn_s_sleep(PD);
            gwait<2>(ws, WS_FA + fo, tag); lset<M_LAND>(vm, 2);
            STAMP(5);
            lwait<M_CB, 4>(vm, 8);
            const rsrc_t rsO = mk(wo, 448 * XROW); const unsigned oo = (lane / 14) * XROW + (lane % 14) * 16;
            __builtin_amdgcn_s_sleep(WOD);
#pragma unroll 1
            for (int k = 0; k < 28; ++k) {
                if (lane < 56) dma<4, 896, 4 * XROW, false>(lds0 + k * 4 * 896, oo, rsO, 224 * cg + k * 16 * XROW);
                WAITV(4); __builtin_amdgcn_s_sleep(WOS);
            }
            const unsigned sk = rwait<2>(rsW, WS_SEL + (lane & 7) * 16, tag);
            *(volatile LAS unsigned*)(misc + M_SEL + (lane & 7) * 4) = sk;
            gwait(ws, WS_FA + (14 * 16 + qb) * FS, tag);
            unsigned mine = (unsigned)__ballot(((0xffff - (sk & 0xffff)) >> 5) == (unsigned)qtile) & 255u;
            if (mine) gwait<8>(ws, WS_FY + ((lane < 28 ? lane : 0) * 4 + cg) * FS, tag);
#pragma unroll 1
            for (; mine; mine &= mine - 1) {
                const int slot = __builtin_ctz(mine), l14 = lane / 14, m14 = lane - l14 * 14; const lptr yb = lds + L_Y + 3584;
                f32x4 a = {}, b = {};
#pragma unroll
                for (int k = 0; k < 7; ++k) { const unsigned yo = WS_Y + (((l14 & 3) * 7 + k) * 4 + cg) * 3584 + slot * 448 + m14 * 32; a += __builtin_bit_cast(f32x4, ld16(rsW, yo)); b += __builtin_bit_cast(f32x4, ld16(rsW, yo + 16)); }
                *(LAS f32x4*)(yb + lane * 32) = a; *(LAS f32x4*)(yb + lane * 32 + 16) = b;
                const unsigned o16 = (unsigned)__builtin_amdgcn_readlane(sk, slot) >> 16, u16 = (o16 & 0x8000u) ? (o16 & 0x7fffu) : (~o16 & 0xffffu); const float rs = rbf(sigm(bflo(u16)));
                if (lane < 28) { const f32x4 t = (*(const LAS f32x4*)(yb + lane * 16) + *(const LAS f32x4*)(yb + 448 + lane * 16)) + (*(const LAS f32x4*)(yb + 896 + lane * 16) + *(const LAS f32x4*)(yb + 1344 + lane * 16));
                    *(LAS f32x4*)(lds + L_Y + slot * 448 + lane * 16) = (f32x4){rbf(rbf(t[0]) * rs), rbf(rbf(t[1]) * rs), rbf(rbf(t[2]) * rs), rbf(rbf(t[3]) * rs)}; }
            }
            WAITV(0); lset<M_AUX>(vm, one); STAMP(7);
            lwait<M_COUT, 2>(vm, 4);
            __builtin_amdgcn_s_sleep(8);
            gwait(ws, WS_FO + (qtile * 16 + (lane & 15)) * FS, tag);
            STAMP(8);
#pragma unroll
            for (int k = 0; k < 4; ++k) { dma<4, 1024, 1024, true>(lds0 + L_CB + k * 4096, vo, rsW, WS_O + qtile * 32768 + k * 4096); dma<4, 1024, 1024, true>(lds0 + L_CB + 16384 + k * 4096, vo, rsW, WS_O + qtile * 32768 + 16384 + k * 4096); }
            WAITV(24); lset<M_OL>(vm, one); WAITV(16); lset<M_OL>(vm, 2); WAITV(8); lset<M_OL>(vm, 3); WAITV(0); lset<M_OL>(vm, 4); STAMP(10);
            if (bid == 0) {
#pragma unroll 1
                for (int k = 0; k < 4; ++k) gwait(ws, WS_FO + ((k * 4 + (lane >> 4)) * 64 + (lane & 15)) * FS, tag);
                gset(ws, WS_EP, tag);
            }
        } else {
            const unsigned qo = (lane >> 2) * WROW + (lane & 3) * 16, qs = 56 * h - 8 * (h & 1);
            dma<12, 1024, 16 * WROW, false>(lds0 + L_WQ, qo, rsWq, qs);
            WAITV(0);
            lset<M_WQ>(vm, one);
            lwait<M_XI, 1>(vm, hasw ? 2 : 0);
            dma<16, 1024, 16 * WROW, false>(lds0 + L_WQ + 12288, qo, rsWq, qs + 192 * WROW);
            WAITV(0); lset<M_WQ>(vm, 2); STAMP(4);
            __builtin_amdgcn_s_setprio(3);
            const u64 tag = ltag(vm);
            if (bid == 234) {
                unsigned k0 = rwait<2>(rsW, WS_CAND + lane * 16, tag), k1 = rwait<2>(rsW, WS_CAND + 1024 + lane * 16, tag), mine = 0;
#pragma unroll
                for (int k = 0; k < 8; ++k) { const unsigned w = wave_umax(umax(k0, k1)); if (k0 == w) k0 = 0; if (k1 == w) k1 = 0; if (lane == k) mine = w; }
                if (lane < 8) st16(rsW, WS_SEL + lane * 16, rec(tag, mine));
                STAMP(7);
            }
            lwait<M_CB, 2>(vm, 8); lwait<M_LAND, 2>(vm, 2);
            const int r = lane & 31, hh = lane >> 5, tq = (lane & 15) >> 2, tp = lane & 3, tg = (lane >> 4) & 1, l14 = lane / 14, m14 = lane - l14 * 14;
            const int wk = bid < 112 ? bid : bid - 112, G = wk >> 2, sub = wk & 3; const lptr st = lds + L_MOD;
            {   const unsigned jk = lds0 + L_MISC + M_JUNK;
                if (PFM & 1) { if (lane < 49) { touch(jk, lane * 128, mk(wg, 448 * FROW), bid * 6272); if (PFM & 4) { WAITV(0); __builtin_amdgcn_s_sleep(PFS); } touch(jk, lane * 128, mk(wu, 448 * FROW), bid * 6272); } }
                if (PFM & 2) { WAITV(0); __builtin_amdgcn_s_sleep(PFS); }
                if (PFM & 1) { if (lane < 49) touch(jk, lane * 128, mk(wd, 448 * FROW), bid * 6272); }
                if (lane < 13) touch(jk, lane * 128, mk(wo, 448 * XROW), bid * 1664);
                if (PFM & 1) { WAITV(0); __builtin_amdgcn_s_sleep(PFS); } }
            if (bid < 112) {
                const unsigned go = (lane >> 3) * FROW + (((lane & 7) ^ ((lane >> 2) & 4)) * 16), so = sub * 112 * FROW + G * 128;
                dma<14, 1024, 8 * FROW, false>(lds0 + L_MOD, go, mk(wg, 448 * FROW), so);
                dma<14, 1024, 8 * FROW, false>(lds0 + L_MOD + 14336, go, mk(wu, 448 * FROW), so);
                const unsigned ks = rwait<8>(rsW, WS_SEL + (lane & 7) * 16, tag), ka_ = __builtin_amdgcn_ds_bpermute((l14 & 3) * 4, ks), kb_ = __builtin_amdgcn_ds_bpermute((l14 & 3) * 4 + 16, ks);
                STAMP(5);
                if (lane < 56) { dma1(lds0 + L_MOD + 28672, ((0xffffu - (ka_ & 0xffffu)) & 2047u) * XROW + m14 * 16, rsX, sub * 224); dma1(lds0 + L_MOD + 28672 + 896, ((0xffffu - (kb_ & 0xffffu)) & 2047u) * XROW + m14 * 16, rsX, sub * 224); }
                WAITV(0); STAMP(8);
                bf16x8 fx[7], fw[7][4]; f32x16 acc[4] = {};
                const lptr wt0 = st + (8 * hh + tq) * 128 + ((tq >> 1) * 64) + 32 * tg + 8 * tp, wt1 = st + (8 * hh + tq) * 128 + (((tq >> 1) ^ 1) * 64) + 32 * tg + 8 * tp;
#define S2RD(s) do { fx[s] = ldsr(st + 28672 + (r & 7) * 224 + (s) * 32 + hh * 16); \
                    _Pragma("unroll") for (int t = 0; t < 4; ++t) { const lptr w_ = ((t & 1) ? wt1 : wt0) + (t >> 1) * 14336 + (s) * 2048; fw[s][t] = ldtr2(w_, w_ + 512); } } while (0)
                S2RD(0); SBAR();
#pragma unroll
                for (int s = 0; s < 7; ++s) { if (s < 6) S2RD(s + 1); SBAR();
#pragma unroll
                    for (int t = 0; t < 4; ++t) acc[t] = MFMA32(fw[s][t], fx[s], acc[t]);
                    SBAR(); }
                if (r < 8) {
#pragma unroll
                    for (int k = 0; k < 16; ++k) { const int t = k >> 2, a = k & 3; *(LAS f32x4*)(st + 30720 + (2 * (t >> 1) + hh) * 1024 + (r + 8 * (4 * (t & 1) + a)) * 16) = (f32x4){acc[t][4 * a], acc[t][4 * a + 1], acc[t][4 * a + 2], acc[t][4 * a + 3]}; } }
#pragma unroll
                for (int k = 0; k < 4; ++k) st16(rsW, WS_P + bid * 4096 + k * 1024 + lane * 16, *(const LAS u32x4*)(st + 30720 + k * 1024 + lane * 16));
                WAITV(0);
                gset(ws, WS_FH + bid * FS, tag); STAMP(9);
            } else if (bid < 224) {
                if (lane < 56) dma<16, 896, 4 * XROW, false>(lds0 + L_MOD, l14 * XROW + m14 * 16, mk(wd, 1792 * XROW), G * 64 * XROW + sub * 224);
                WAITV(0); __builtin_amdgcn_s_sleep(32);
                gwait<8>(ws, WS_FH + (G * 4 + tp) * FS, tag);
                STAMP(10);
                u32x4 pr[16]; bf16x8 fw[4][4];
#pragma unroll
                for (int k = 0; k < 16; ++k) pr[k] = ld16(rsW, WS_P + G * 16384 + k * 1024 + lane * 16);
                const lptr wt = st + (8 * hh + tq) * 224 + 32 * tg + 8 * tp;
#pragma unroll
                for (int k = 0; k < 16; ++k) fw[k >> 2][k & 3] = ldtr2(wt + 56 * (k & 3) + (k >> 2) * 3584, wt + 56 * (k & 3) + (k >> 2) * 3584 + 896);
                SBAR();
                f32x4 p[4];
#pragma unroll
                for (int k = 0; k < 4; ++k) p[k] = (__builtin_bit_cast(f32x4, pr[k]) + __builtin_bit_cast(f32x4, pr[4 + k])) + (__builtin_bit_cast(f32x4, pr[8 + k]) + __builtin_bit_cast(f32x4, pr[12 + k]));
                float hv[8];
#pragma unroll
                for (int e = 0; e < 8; ++e) { const float gv = rbf(p[e >> 2][e & 3]); hv[e] = rbf(gv * sigm(gv)) * rbf(p[2 + (e >> 2)][e & 3]); }
                *(LAS u32x4*)(st + 16384 + lane * 16) = (u32x4){cvtpk(hv[0], hv[1]), cvtpk(hv[2], hv[3]), cvtpk(hv[4], hv[5]), cvtpk(hv[6], hv[7])};
                bf16x8 fh[4]; f32x16 acc[4] = {};
#pragma unroll
                for (int s = 0; s < 4; ++s) fh[s] = ldsr(st + 16384 + ((2 * s + hh) * 8 + (r & 7)) * 16);
#pragma unroll
                for (int k = 0; k < 16; ++k) acc[k & 3] = MFMA32(fw[k >> 2][k & 3], fh[k >> 2], acc[k & 3]);
                if (r < 8) {
#pragma unroll
                    for (int k = 0; k < 16; ++k) { const int t = k >> 2, a = k & 3; if (a < 3 || hh == 0) *(LAS f32x4*)(st + 18432 + r * 448 + (28 * t + 8 * a + 4 * hh) * 4) = (f32x4){acc[t][4 * a], acc[t][4 * a + 1], acc[t][4 * a + 2], acc[t][4 * a + 3]}; } }
#pragma unroll
                for (int k = 0; k < 4; ++k) if (k < 3 || lane < 32) st16(rsW, WS_Y + wk * 3584 + k * 1024 + lane * 16, *(const LAS u32x4*)(st + 18432 + k * 1024 + lane * 16));
                WAITV(0);
                gset(ws, WS_FY + wk * FS, tag);
            }
            STAMP(6);
            lset<M_WO>(vm, one);
            STAMP(12);
        }
        STAMP_FLUSH();
        return;
    }

    u64 tag;
    {
        const int r = lane & 31, hh = lane >> 5;
        const int tq = (lane & 15) >> 2, tg = (lane >> 4) & 1, ch = wid & 1, swz = (r >> 1) & 7;
        lptr xp[4];
#pragma unroll
        for (int k = 0; k < 4; ++k) xp[k] = lds + xq(0) + r * 128 + (((2 * k + hh) ^ swz) * 16);
        const lptr v4 = vm + (lane & 3) * 4;
        bf16x8 wf[28]; f32x16 acc = {};
        if (__builtin_expect(wid < 4, 1)) {
            const lptr wa = lds + L_W + (8 * hh + tq) * 128 + ((ch ^ (tq >> 1)) * 64) + 32 * tg + 8 * (lane & 3);
            unsigned d0, d1; u64 m24; bool two0, two1;
            { const int cc = 8 * c + 4 * ch + hh, hp = (cc * 37) >> 8, u = cc - 7 * hp, cd = cc + 2, hq = (cd * 37) >> 8, v = cd - 7 * hq, gv = (c - 7) * 64 + 32 * ch + r, head = (gv * 2341) >> 16, d = gv - head * 28;
              const unsigned dv = head * 262144 + 2048 + (hh * 32 + d) * 16, base = WS_KV + qb * 16384;
              d0 = base + (c >= 7 ? dv : (2 * hp + (u > 3)) * 262144 + (u > 3 ? u - 3 : u) * 512 + r * 16); d1 = base + (c >= 7 ? dv + 1024 : (2 * hq + (v > 3)) * 262144 + (v > 3 ? v - 3 : v) * 512 + r * 16);
              two0 = c >= 7 ? d >= 24 : u == 3; two1 = c >= 7 ? d >= 24 : v == 3; m24 = __ballot(d == 24); PIN(d0); PIN(d1); }
#define WRD(S0, S1) do { _Pragma("unroll") for (int s = S0; s < S1; ++s) wf[s] = ldtr2(wa + s * 2048, wa + s * 2048 + 512); } while (0)
#define KOUT(p, d, two) do { st16(rsW, d, (u32x4){p[0], p[1], two ? 0x3F80u : p[2], two ? 0u : p[3]}); if (two) st16(rsW, d + 262144 - 1536, (u32x4){0x3F80u, 0u, p[2], p[3]}); } while (0)
#define KVOUT(KT) do { const unsigned e0 = d0, e1 = d1; \
                if (KT) { u32x4 p0, p1; ct_to_chunks(acc, 1.f, p0, p1); KOUT(p0, e0, two0); KOUT(p1, e1, two1); } \
                else { st16(rsW, e0, (u32x4){cvtpk(acc[0], acc[1]), cvtpk(acc[2], acc[3]), cvtpk(acc[4], acc[5]), cvtpk(acc[6], acc[7])}); \
                       st16(rsW, e1, (u32x4){cvtpk(acc[8], acc[9]), cvtpk(acc[10], acc[11]), cvtpk(acc[12], acc[13]), cvtpk(acc[14], acc[15])}); \
                       unsigned pad; asm volatile("v_mov_b32 %0, 0x3F803F80\n\tv_cndmask_b32 %0, 0, %0, %1" : "=&v"(pad) : "s"(m24)); if (two0) { st16(rsW, e0 + 64, (u32x4){pad, pad, pad, pad}); st16(rsW, e1 + 64, (u32x4){pad, pad, pad, pad}); } } } while (0)
#define KVPATH(KT) do { int q4 = 1; const lptr pp = lds + L_PP + ch * 4096 + lane * 16; \
                lwait<M_F>(v4, 1); STAMP(12); \
                if (wid < 2) { \
                    mma_steps<KT, 0, 16, 4, 2048>(acc, wf, xp, 0, wa); \
                    _Pragma("unroll") for (int a = 0; a < 4; ++a) *(LAS f32x4*)(pp + a * 1024) = (f32x4){acc[4 * a], acc[4 * a + 1], acc[4 * a + 2], acc[4 * a + 3]}; \
                    ladd<M_P>(vm, one); \
                    lwait<M_F + 16>(v4, 2); WRD(16, 28); ladd<M_CW>(vm, one); tag = ltag(vm); \
                } else { \
                    WRD(0, 16); \
                    lwait<M_F + 16>(v4, 1); mma_steps<KT, 16, 24, 4, 2048>(acc, wf, xp, 0, wa); \
                    lwait<M_F + 16>(v4, 2); STAMP(13); mma_steps<KT, 24, 28, 4, 2048>(acc, wf, xp, 0, wa); ladd<M_CW>(vm, one); \
                    lwait<M_P>(vm, 2); \
                    _Pragma("unroll") for (int a = 0; a < 4; ++a) { const f32x4 t = *(const LAS f32x4*)(pp + a * 1024); acc[4 * a] += t[0]; acc[4 * a + 1] += t[1]; acc[4 * a + 2] += t[2]; acc[4 * a + 3] += t[3]; } \
                    KVOUT(KT); STAMP(14); tag = ltag(vm); WAITV(0); STAMP(2); \
                    if (laddr<M_CQ0>(vm, one) == 1) { gset(ws, WS_FQ0 + (c * 16 + qb) * FS, tag); lset<M_Q0S>(vm, one); } \
                    q4 = 2; \
                } \
                __builtin_amdgcn_s_setprio(3); \
                _Pragma("unroll 1") for (; q4 < 4; q4 += 2) { \
                    _Pragma("unroll") for (int k = 0; k < 4; ++k) { xp[k] += (q4 == 1 ? 1 : -2) * 28672; PIN(xp[k]); } \
                    d0 += (q4 == 1 ? 1 : 2) * 4096; d1 += (q4 == 1 ? 1 : 2) * 4096; \
                    lwait<M_X1 - 4>(vm + 4 * q4, 2); \
                    acc = (f32x16){}; \
                    mma_steps<KT, 0, 28, 3, 0>(acc, wf, xp, 0, wa); if (q4 > 1) ladd<M_CQ>(vm, one); \
                    KVOUT(KT); \
                } __builtin_amdgcn_s_setprio(0); } while (0)
            WAITV(0); STAMP(9); lset<M_F>(vm + wid * 4, one);
            if (__builtin_expect(hasw, 1)) {
                if (c < 7) KVPATH(true); else KVPATH(false);
                WAITV(0);
                if (laddr<M_CA>(vm, one) == 3) { gset(ws, WS_FA + (c * 16 + qb) * FS, tag); lset<M_RS>(vm, one); }
            } else tag = ltag(vm);
        } else {
            const int qt = wid & 3;
            WAITV(3); lset<M_F>(vm + wid * 4, one); WAITV(0); STAMP(9); lset<M_F>(vm + wid * 4, 2);
#define XHALF(q4) do { const unsigned m_ = lds0 + xq(q4) + ch * 2048, s_ = xb + (32 * (q4) + 16 * ch) * XROW; dma<7, 4096, 128, false>(m_, xe, rsX, s_); dma<7, 4096, 128, false>(m_ + 1024, xd, rsX, s_ + 8 * XROW); } while (0)
            if (wid < 6) { XHALF(1); tag = ltag(vm); WAITV(0); ladd<M_X1>(vm, one); }
            else { tag = ltag(vm);
                if (__builtin_expect(hasw, 1)) { lwait<M_CW>(vm, 4); STAMP(2);
                    XHALF(2); XHALF(3); ladd<M_XI>(vm, one); WAITV(14); ladd<M_X2>(vm, one); WAITV(0); ladd<M_X3>(vm, one); } }
            lwait<M_F>(v4, 1); lwait<M_F + 16>(v4, 2);
            lwait<M_X1 - 4>(vm + 4 * (hasw && qt ? qt : 1), 2);
#pragma unroll
            for (int k = 0; k < 4; ++k) { xp[k] += xq(qt) - xq(0); PIN(xp[k]); }
            __builtin_amdgcn_s_setprio(2);
            if (c == 14) {
                const lptr ar = lds + L_QI + (lane & 1) * 1040 + hh * 16;
                lwait<M_LV>(vm, 1);
                mma_steps<true, 0, 28, 6, -1>(acc, wf, xp, 0, ar);
                if (hh == 0) { const int tl = 32 * qt + r, tok = qb * 128 + tl;
                    const unsigned u = cvtpk(acc[0], acc[0]) & 0xffffu;
                    unsigned o = (u & 0x8000u) ? (~u & 0xffffu) : (u | 0x8000u); if ((u & 0x7fffu) > 0x7f80u) o = 0;
                    *(LAS unsigned*)(misc + M_KEYS + tl * 4) = (o << 16) | (unsigned)(0xffff - tok);
                    __builtin_amdgcn_raw_buffer_store_b32(__float_as_uint(rbf(sigm(rbf(acc[1])))), rsW, WS_G + tok * 4, 0, 16); }
                ladd<M_CKEY>(vm, one);
                acc = (f32x16){};
            }
            const lptr wb = lds + L_WQ + (8 * hh + tq) * 64 + 32 * tg + 8 * (lane & 3);
            lwait<M_WQ>(vm, 1); STAMP(3);
            mma_steps<true, 0, 12, 5, 1024>(acc, wf, xp, 0, wb);
            lwait<M_WQ>(vm, 2);
            mma_steps<true, 12, 28, 5, 1024>(acc, wf, xp, 0, wb);
            u32x4 p0, p1; ct_to_chunks(acc, QSCALE, p0, p1);
            const bool z0 = (h & 1) && !hh, z1 = !(h & 1) && hh;
            if (c == 14) lwait<M_CKEY>(vm, 4);
            *(LAS u32x4*)(lds + L_QI + qt * 2048 + lane * 16) = (u32x4){z0 ? 0u : p0[0], z0 ? 0u : p0[1], p0[2], p0[3]};
            *(LAS u32x4*)(lds + L_QI + qt * 2048 + 1024 + lane * 16) = (u32x4){p1[0], p1[1], z1 ? 0u : p1[2], z1 ? 0u : p1[3]};
            ladd<M_CQI>(vm, one); ladd<M_CQ>(vm, one);
            if (!hasw) { WAITV(0); if (laddr<M_CA>(vm, one) == 3) { gset(ws, WS_FA + (c * 16 + qb) * FS, tag); lset<M_RS>(vm, one); } }
            __builtin_amdgcn_s_setprio(0);
        }
        if (c == 14 && wid < 4) {
            lwait<M_CKEY>(vm, 4);
            asm volatile("v_mbcnt_lo_u32_b32 %0, -1, 0\n\tv_mbcnt_hi_u32_b32 %0, -1, %0" : "=v"(tid)); tid += wid * 64;
            const unsigned key = *(const LAS unsigned*)(misc + M_KEYS + (tid >> 1) * 4); unsigned cnt = 0;
#pragma unroll 4
            for (int j = 0; j < 16; ++j) { const u32x4 v = *(const LAS u32x4*)(misc + M_KEYS + (tid & 1) * 256 + j * 16); cnt += (v[0] > key) + (v[1] > key) + (v[2] > key) + (v[3] > key); }
            cnt += __builtin_amdgcn_update_dpp(0u, cnt, 0xB1, 0xf, 0xf, true);
            if ((tid & 1) == 0 && cnt < 8) st16(rsW, WS_CAND + (qb * 8 + cnt) * 16, rec(tag, key));
        }
        STAMP(1);
    }
    STAMP(4);
    asm volatile("v_mbcnt_lo_u32_b32 %0, -1, 0\n\tv_mbcnt_hi_u32_b32 %0, -1, %0" : "=v"(lane));
    __builtin_assume(lane >= 0 && lane < 64);
    tid = wid * 64 + lane;
    const int r = lane & 31, hh = lane >> 5;

    const int qp = wid & 1, kq = wid >> 1;
    f32x16 o0, o1; float m0, m1;
    lptr cb = lds + L_CB + qp * 24576 + lane * 16, ml = lds + L_ML + qp * 2048 + r * 16;
    unsigned od = WS_O + ((qb * 4 + 2 * qp + (kq >> 1)) * 16 + h) * 2048 + (kq & 1) * 1024 + lane * 16;
    PIN(ml); PIN(cb); PIN(od);
    unsigned co0 = ((kq - 1) & 3) * 2048, co1 = ((kq - 2) & 3) * 2048, co2 = ((kq - 3) & 3) * 2048, co3 = kq * 2048, crd = kq * 6144, cc4 = qp * 4, cw0 = kq * 512, ck8 = kq * 8;
    unsigned cw1 = ((kq + 1) & 3) * 512 + (kq >> 1) * 8, cw2 = ((kq + 2) & 3) * 512 + (kq >> 1) * 8, cw3 = ((kq + 3) & 3) * 512 + (kq >> 1) * 8;
    const unsigned q1 = RFL(0u - (unsigned)(kq >> 1)); u64 zm = (u64)(unsigned)RFL(0u - (unsigned)(kq & 1)) << 32, qm = q1 | ((u64)q1 << 32);
    asm volatile("" : "+s"(co0), "+s"(co1), "+s"(co2), "+s"(co3), "+s"(crd), "+s"(cc4), "+s"(cw0), "+s"(ck8), "+s"(cw1), "+s"(cw2), "+s"(cw3), "+s"(zm), "+s"(qm));
    {
        const rsrc_t rsH = mk(ws + WS_KV + h * 262144, 262144); unsigned vo = kq * 16384 + lane * 16, s0 = 131072, s1 = 65536, n, mb;
        const u64 mo = (h & 1) ? 0x00000000ffffffffull : 0, me = (h & 1) ? 0 : 0xffffffff00000000ull;
        lwait<M_CQI>(vm, 4);
        const lptr qi = lds + L_QI + qp * 4096 + lane * 16;
        u32x4 qa = *(const LAS u32x4*)qi, qb_ = *(const LAS u32x4*)(qi + 1024), qc = *(const LAS u32x4*)(qi + 2048), qd = *(const LAS u32x4*)(qi + 3072);
        lwait<GATE>(vm, GATEN); lwait<M_LAND>(vm, 1);
        STAMP(5);
#define LD2(r, so, o) "buffer_load_dwordx4 v[" #r ":" #r "+3], %[vo], %[rs], " so " offen offset:" #o " sc1\n\tbuffer_load_dwordx4 v[" #r "+4:" #r "+7], %[vo], %[rs], " so " offen offset:" #o "+1024 sc1\n\t"
#define MAX16(d, e, s) "v_max3_f32 v" #d ", v[" #s "], v[" #s "+1], v[" #s "+2]\n\tv_max3_f32 v" #e ", v[" #s "+3], v[" #s "+4], v[" #s "+5]\n\tv_max3_f32 v" #d ", v" #d ", v[" #s "+6], v[" #s "+7]\n\t" \
            "v_max3_f32 v" #e ", v" #e ", v[" #s "+8], v[" #s "+9]\n\tv_max3_f32 v" #d ", v" #d ", v[" #s "+10], v[" #s "+11]\n\tv_max3_f32 v" #e ", v" #e ", v[" #s "+12], v[" #s "+13]\n\t" \
            "v_max3_f32 v" #d ", v" #d ", v[" #s "+14], v[" #s "+15]\n\tv_max_f32 v" #d ", v" #d ", v" #e "\n\t"
        asm volatile(LD2(80, "0", 0) LD2(96, "0", 2048) LD2(88, "%[s1]", 0)
                     "s_waitcnt vmcnt(4)\n\t"
                     "v_mfma_f32_32x32x16_bf16 v[0:15], v[80:83], v[64:67], 0\n\tv_mfma_f32_32x32x16_bf16 v[16:31], v[80:83], v[72:75], 0\n\t"
                     "v_mfma_f32_32x32x16_bf16 v[0:15], v[84:87], v[68:71], v[0:15]\n\tv_mfma_f32_32x32x16_bf16 v[16:31], v[84:87], v[76:79], v[16:31]\n\t"
                     "s_mov_b64 exec, 1\n\tds_add_u32 %[vm], %[one] offset:%c[cb]\n\ts_mov_b64 exec, -1\n\t" LD2(104, "%[s1]", 2048) LD2(132, "%[s0]", 0) "s_nop 3\n\t"
                     MAX16(128, 129, 0) MAX16(130, 131, 16)
                     "s_nop 1\n\t"
                     "v_permlane32_swap_b32 v128, v130\n\tv_max_f32 v128, v128, v130\n\tv_mov_b32 v130, v128\n\ts_nop 1\n\tv_permlane32_swap_b32 v128, v130\n\t"
                     "v_cvt_pk_bf16_f32 %[mb], -v128, -v130\n\tv_and_b32 v128, 0xffff, %[mb]\n\tv_lshrrev_b32 v129, 16, %[mb]\n\t"
                     "v_cndmask_b32 v64, v64, v128, %[mo]\n\tv_cndmask_b32 v70, v70, v128, %[me]\n\tv_cndmask_b32 v72, v72, v129, %[mo]\n\tv_cndmask_b32 v78, v78, v129, %[me]\n\ts_nop 1\n\t"
                     "v_mfma_f32_32x32x16_bf16 v[0:15], v[80:83], v[64:67], 0\n\tv_mfma_f32_32x32x16_bf16 v[0:15], v[84:87], v[68:71], v[0:15]\n\ts_nop 9\n\t" ATTN_LOOP
                     : "=&{v[32:47]}"(o0), "=&{v[48:63]}"(o1), "+{v[64:67]}"(qa), "+{v[68:71]}"(qb_), "+{v[72:75]}"(qc), "+{v[76:79]}"(qd), [mb] "=&v"(mb), [n] "=&s"(n), [s0] "+&s"(s0), [s1] "+&s"(s1)
                     : [vo] "v"(vo), [rs] "s"(rsH), [vm] "v"(vm), [one] "v"(one), [ld] "v"(vm + M_LAND), [mo] "s"(mo), [me] "s"(me), [cb] "i"(M_CB)
                     : "memory", "scc", "vcc", "v0", "v1", "v2", "v3", "v4", "v5", "v6", "v7", "v8", "v9", CL10(1), CL10(2), "v30", "v31", CL10(8), CL10(9), CL10(10), CL10(11), CL10(12), CL10(13),
                       "v140", "v141", "v142", "v143", "v144", "v145", "v146", "v147", "v148");
        m0 = -bflo(mb); m1 = -bfhi(mb);
        const f32x16 zero = {};
        const unsigned ok = (unsigned)((__ballot(o0[12] >= 1.0e-18f) & __ballot(o0[12] <= 1.0e18f) & __ballot(o1[12] >= 1.0e-18f) & __ballot(o1[12] <= 1.0e18f)) >> 32);
        if (__builtin_expect(RFL(ok) != 0xffffffffu, 0)) {
            if (h & 1) { qa[0] = hh ? qa[0] : 0u; qc[0] = hh ? qc[0] : 0u; } else { qb_[2] = hh ? 0u : qb_[2]; qd[2] = hh ? 0u : qd[2]; }
            const unsigned kb = WS_KV + h * 262144 + kq * 16384 + lane * 16;
#pragma unroll 1
            for (int i = 0; i < 2; ++i) { const bf16x8 qx = BC8(i ? qc : qa), qy = BC8(i ? qd : qb_); float mm = -INFINITY; f32x16 o = zero;
#pragma unroll 1
                for (int n2 = 0; n2 < 16; ++n2) { const unsigned a = kb + (n2 >> 2) * 65536 + (n2 & 3) * 4096;
                    f32x16 s = MFMA32(BC8(ld16(rsW, a)), qx, zero); s = MFMA32(BC8(ld16(rsW, a + 1024)), qy, s); mm = __builtin_fmaxf(mm, max16(s)); }
                mm = halfmax(mm);
#pragma unroll 1
                for (int n2 = 0; n2 < 16; ++n2) { const unsigned a = kb + (n2 >> 2) * 65536 + (n2 & 3) * 4096;
                    f32x16 s = MFMA32(BC8(ld16(rsW, a)), qx, zero); s = MFMA32(BC8(ld16(rsW, a + 1024)), qy, s);
#pragma unroll
                    for (int e = 0; e < 16; ++e) s[e] = EX(s[e] - mm);
                    o = MFMA32(BC8(ld16(rsW, a + 2048)), BC8(((u32x4){cvtpk(s[0], s[1]), cvtpk(s[2], s[3]), cvtpk(s[4], s[5]), cvtpk(s[6], s[7])})), o);
                    o = MFMA32(BC8(ld16(rsW, a + 3072)), BC8(((u32x4){cvtpk(s[8], s[9]), cvtpk(s[10], s[11]), cvtpk(s[12], s[13]), cvtpk(s[14], s[15])})), o); }
                if (i) { o1 = o; m1 = mm; } else { o0 = o; m0 = mm; } }
            asm volatile("" : "+{v[32:47]}"(o0), "+{v[48:63]}"(o1));
        }
    }
    STAMP(6);
    {
        const int qt = 2 * qp + (kq >> 1); unsigned n;
#define PUT(j, a) "s_cmp_eq_u32 %[kq], " #j "\n\ts_cselect_b64 exec, 0, -1\n\tds_write_b128 v" #a ", v[32+8*" #j ":35+8*" #j "] offset:6144*" #j "\n\tds_write_b128 v" #a ", v[36+8*" #j ":39+8*" #j "] offset:6144*" #j "+1024\n\t"
#define SPIN(L, a, off, need) #L ":\n\tds_read_b32 v8, " a " offset:" off "\n\ts_sub_u32 %[n], %[n], 1\n\ts_waitcnt lgkmcnt(0)\n\ts_cbranch_scc1 " #L "0f\n\tv_cmp_gt_u32 vcc, " #need ", v8\n\ts_cbranch_vccnz " #L "b\n" #L "0:\n\t"
#define ACC(f, u) "v_pk_fma_f32 v[0:1], v[" #u ":" #u "+1], v[" #f ":" #f "+1], v[0:1] op_sel_hi:[1,0,1]\n\tv_pk_fma_f32 v[2:3], v[" #u "+2:" #u "+3], v[" #f ":" #f "+1], v[2:3] op_sel_hi:[1,0,1]\n\t" \
                  "v_pk_fma_f32 v[4:5], v[" #u "+4:" #u "+5], v[" #f ":" #f "+1], v[4:5] op_sel_hi:[1,0,1]\n\tv_pk_fma_f32 v[6:7], v[" #u "+6:" #u "+7], v[" #f ":" #f "+1], v[6:7] op_sel_hi:[1,0,1]\n\t"
        asm volatile("v_mov_b32 v65, v44\n\tv_mov_b32 v67, v60\n\tv_mov_b32 v68, v44\n\tv_mov_b32 v69, v60\n\tv_mov_b32 v64, %[m0]\n\tv_mov_b32 v66, %[m1]\n\t"
                     "v_permlane32_swap_b32 v68, v65\n\tv_permlane32_swap_b32 v69, v67\n\t"
                     "v_add_u32 v70, %[o0], %[cb]\n\tv_add_u32 v71, %[o1], %[cb]\n\tv_add_u32 v72, %[o2], %[cb]\n\tv_add_u32 v73, %[o3], %[cb]\n\tv_add_u32 v74, %[w0], %[ml]\n\tv_add_u32 v79, %[c4], %[vm]\n\t"
                     "s_movk_i32 %[n], 0x7fff\n" SPIN(1, "%[vm]", "%c[wo]", 1)
                     PUT(0, 70) PUT(1, 71) PUT(2, 72) PUT(3, 73)
                     "s_mov_b64 exec, -1\n\tds_write_b128 v74, v[64:67]\n\ts_mov_b64 exec, 1\n\tds_add_u32 v79, %[one] offset:%c[cp]\n\ts_mov_b64 exec, -1\n\t"
                     "s_set_gpr_idx_on %[k8], gpr_idx(SRC0)\n\tv_mov_b32 v0, v32\n\tv_mov_b32 v1, v33\n\tv_mov_b32 v2, v34\n\tv_mov_b32 v3, v35\n\tv_mov_b32 v4, v36\n\tv_mov_b32 v5, v37\n\tv_mov_b32 v6, v38\n\tv_mov_b32 v7, v39\n\ts_set_gpr_idx_off\n\t"
                     "v_cndmask_b32 v64, v64, v66, %[qm]\n\tv_cndmask_b32 v65, v65, v67, %[qm]\n\t"
                     "v_add_u32 v75, %[w1], %[ml]\n\tv_add_u32 v76, %[w2], %[ml]\n\tv_add_u32 v77, %[w3], %[ml]\n\tv_add_u32 v78, %[rd], %[cb]\n\t"
                     "s_movk_i32 %[n], 0x7fff\n" SPIN(2, "v79", "%c[cp]", 4)
                     "ds_read_b64 v[66:67], v75\n\tds_read_b64 v[68:69], v76\n\tds_read_b64 v[70:71], v77\n\t"
                     "ds_read_b128 v[8:11], v78\n\tds_read_b128 v[12:15], v78 offset:1024\n\tds_read_b128 v[16:19], v78 offset:2048\n\tds_read_b128 v[20:23], v78 offset:3072\n\tds_read_b128 v[24:27], v78 offset:4096\n\tds_read_b128 v[28:31], v78 offset:5120\n\t"
                     "s_waitcnt lgkmcnt(6)\n\tv_max3_f32 v72, v64, v66, v68\n\tv_max_f32 v72, v72, v70\n\t"
                     "v_sub_f32 v64, v64, v72\n\tv_sub_f32 v66, v66, v72\n\tv_sub_f32 v68, v68, v72\n\tv_sub_f32 v70, v70, v72\n\tv_exp_f32 v64, v64\n\tv_exp_f32 v66, v66\n\tv_exp_f32 v68, v68\n\tv_exp_f32 v70, v70\n\t"
                     "v_mul_f32 v72, v64, v65\n\tv_fmac_f32 v72, v66, v67\n\tv_fmac_f32 v72, v68, v69\n\tv_fmac_f32 v72, v70, v71\n\tv_rcp_f32 v72, v72\n\ts_nop 0\n\t"
                     "v_mul_f32 v64, v64, v72\n\tv_mul_f32 v66, v66, v72\n\tv_mul_f32 v68, v68, v72\n\tv_mul_f32 v70, v70, v72\n\t"
                     "v_pk_mul_f32 v[0:1], v[0:1], v[64:65] op_sel_hi:[1,0]\n\tv_pk_mul_f32 v[2:3], v[2:3], v[64:65] op_sel_hi:[1,0]\n\tv_pk_mul_f32 v[4:5], v[4:5], v[64:65] op_sel_hi:[1,0]\n\tv_pk_mul_f32 v[6:7], v[6:7], v[64:65] op_sel_hi:[1,0]\n\t"
                     "s_waitcnt lgkmcnt(4)\n\t" ACC(66, 8) "s_waitcnt lgkmcnt(2)\n\t" ACC(68, 16) "s_waitcnt lgkmcnt(0)\n\t" ACC(70, 24)
                     "v_cvt_pk_bf16_f32 v0, v0, v1\n\tv_cvt_pk_bf16_f32 v1, v2, v3\n\tv_cvt_pk_bf16_f32 v2, v4, v5\n\tv_cvt_pk_bf16_f32 v3, v6, v7\n\t"
                     "v_cndmask_b32 v2, v2, 0, %[zm]\n\tv_cndmask_b32 v3, v3, 0, %[zm]\n\tbuffer_store_dwordx4 v[0:3], %[od], %[rs], 0 offen sc1\n\ts_nop 1"
                     : [n] "=&s"(n)
                     : "{v[32:47]}"(o0), "{v[48:63]}"(o1), [m0] "v"(m0), [m1] "v"(m1), [cb] "v"(cb), [ml] "v"(ml), [vm] "v"(vm), [one] "v"(one), [od] "v"(od), [rs] "s"(rsW), [kq] "s"(kq), [k8] "s"(ck8),
                       [o0] "s"(co0), [o1] "s"(co1), [o2] "s"(co2), [o3] "s"(co3), [rd] "s"(crd), [c4] "s"(cc4), [w0] "s"(cw0), [w1] "s"(cw1), [w2] "s"(cw2), [w3] "s"(cw3), [zm] "s"(zm), [qm] "s"(qm), [wo] "i"(M_WO), [cp] "i"(M_CP)
                     : "memory", "scc", "vcc", "v0", "v1", "v2", "v3", "v4", "v5", "v6", "v7", "v8", "v9", CL10(1), CL10(2), "v30", "v31", "v64", "v65", "v66", "v67", "v68", "v69", CL10(7));
        STAMP(14);
        WAITV(0);
        if (laddr<M_CO>(vm + qt * 4, one) == 1) { gset(ws, WS_FO + ((qb * 4 + qt) * 16 + h) * FS, tag); ladd<M_COUT>(vm, one); }
    }
    STAMP(7);

    const int ct = wid & 3, kh = wid >> 2;
    const int erow = tid / 14, ech = tid - erow * 14, tok = qtile * 32 + erow, ecol = cg * 112 + ech * 8; const bool live = tid < 448;
    u32x4 xv = {}; float g = 0.f, y[8] = {};
    if (live) xv = __builtin_amdgcn_raw_buffer_load_b128(rsX, tok * XROW + ecol * 2, 0, 0);
    bf16x8 wf[16];
    {
        const int tq = (lane & 15) >> 2, tp = lane & 3, tg = (lane >> 4) & 1;
        const lptr wa = lds + (kh * 224 + 4 * hh + tq) * 224 + 56 * ct + 32 * tg + 8 * tp, wz = wa - hh * 896;
        lwait<M_AUX>(vm, 1);
#pragma unroll
        for (int j = 0; j < 8; ++j) { wf[2 * j] = ldtr2(wa + (28 * j) * 224, wa + (28 * j + 8) * 224); wf[2 * j + 1] = ldtr2(wa + (28 * j + 16) * 224, wz + (28 * j + 24) * 224); }
    }
    if (live) {
        g = __uint_as_float(__builtin_amdgcn_raw_buffer_load_b32(rsW, WS_G + tok * 4, 0, 16));
        const u32x4 sa = *(const LAS u32x4*)(misc + M_SEL), sb = *(const LAS u32x4*)(misc + M_SEL + 16);
        int slot = -1;
#pragma unroll
        for (int k = 0; k < 8; ++k) { const unsigned s = k < 4 ? sa[k & 3] : sb[k & 3]; if ((int)(0xffff - (s & 0xffff)) == tok) slot = k; }
        if (slot >= 0) { const f32x4 a = *(const LAS f32x4*)(lds + L_Y + slot * 448 + ech * 32), b = *(const LAS f32x4*)(lds + L_Y + slot * 448 + ech * 32 + 16);
#pragma unroll
            for (int e = 0; e < 4; ++e) { y[e] = a[e]; y[4 + e] = b[e]; } }
    }
    float bb[8]; const float og = rbf(1.f - g);
#pragma unroll
    for (int e = 0; e < 8; ++e) { bb[e] = og * (((e & 1) ? bfhi(xv[e >> 1]) : bflo(xv[e >> 1])) + y[e]); PIN(bb[e]); }
#pragma unroll
    for (int j = 0; j < 16; ++j) PIN(wf[j]);
    ladd<M_CC>(vm, one);
    STAMP(8);
    f32x16 acc = {};
    {
        const lptr ob = lds + L_CB + kh * 16384 + lane * 16;
#pragma unroll
        for (int k = 0; k < 4; ++k) { lwait<M_OL>(vm, k + 1); if (k == 0) STAMP(10);
            const bf16x8 b0 = ldsr(ob + k * 4096), b1 = ldsr(ob + k * 4096 + 1024), b2 = ldsr(ob + k * 4096 + 2048), b3 = ldsr(ob + k * 4096 + 3072); SBAR();
            acc = MFMA32(wf[4 * k], b0, acc); acc = MFMA32(wf[4 * k + 1], b1, acc); acc = MFMA32(wf[4 * k + 2], b2, acc); acc = MFMA32(wf[4 * k + 3], b3, acc); SBAR(); }
    }
    lwait<M_CC>(vm, 8);
#pragma unroll
    for (int a = 0; a < 4; ++a) if (a < 3 || hh == 0) *(LAS f32x4*)(lds + kh * 14848 + r * 464 + (28 * ct + 8 * a + 4 * hh) * 4) = (f32x4){acc[4 * a], acc[4 * a + 1], acc[4 * a + 2], acc[4 * a + 3]};
    LBAR();
    if (live) {
        const lptr pr = lds + erow * 464 + ech * 32;
        const f32x4 a0 = *(const LAS f32x4*)pr + *(const LAS f32x4*)(pr + 14848), a1 = *(const LAS f32x4*)(pr + 16) + *(const LAS f32x4*)(pr + 14848 + 16);
        __builtin_amdgcn_raw_buffer_store_b128((u32x4){cvtpk(g * a0[0] + bb[0], g * a0[1] + bb[1]), cvtpk(g * a0[2] + bb[2], g * a0[3] + bb[3]), cvtpk(g * a1[0] + bb[4], g * a1[1] + bb[5]), cvtpk(g * a1[2] + bb[6], g * a1[3] + bb[7])}, mk(out, 2048 * XROW), tok * XROW + ecol * 2, 0, 0);
    }
    STAMP(11);
    STAMP_FLUSH();
}

extern "C" void kernel_launch(void* const* d_in, const int* in_sizes, int n_in, void* d_out, int out_size, void* d_ws, size_t ws_size, hipStream_t stream) {
    (void)hipFuncSetAttribute((const void*)fused, hipFuncAttributeMaxDynamicSharedMemorySize, L_BYTES);
    fused<<<256, 640, L_BYTES, stream>>>((const char*)d_in[0], (const char*)d_in[1], (const char*)d_in[2], (const char*)d_in[4], (const char*)d_in[5], (const char*)d_in[6], (const char*)d_in[7],
                                          (const char*)d_in[8], (char*)d_ws, (char*)d_out);
}
```
